```python
import torch

def _softmax(x, axis=-1):
    return torch.softmax(x, dim=axis)
def _gelu(x):
    return torch.nn.functional.gelu(x, approximate="tanh")
def _silu(x):
    return torch.nn.functional.silu(x)
def _sigmoid(x):
    return torch.sigmoid(x)
def _layernorm(x, axis=-1, eps=1e-5):
    m = x.mean(dim=axis, keepdim=True)
    v = x.var(dim=axis, keepdim=True, unbiased=False)
    return (x - m) / torch.sqrt(v + eps)
def _scatter_add(tgt, idx, src, axis=0):
    return tgt.clone().index_add_(axis, idx, src)
def _segment_sum(src, seg, n):
    out = torch.zeros((n,) + src.shape[1:], dtype=src.dtype)
    return out.index_add_(0, seg, src)
def _pad(x, pw):
    flat = tuple(v for pair in reversed(pw) for v in pair)
    return torch.nn.functional.pad(x, flat)

kernel_name = 'standalone__w4a16_gemm_dequant__prompt'

def setup_inputs():

    _w = (lambda a: (lambda t: t.to(torch.bfloat16) if t.is_floating_point() else t)(torch.as_tensor(a)))
    B, S, D, F = 1, 2048, 1024, 3584
    G = 128 if 128 > 0 else D
    D = (D // G) * G
    ng = D // G
    qw = torch.randint(0, 16, (D, F)).to(torch.int32)
    return {
        "x": _w((lambda *s: torch.randn(*s, dtype=torch.bfloat16) * 0.05)(B, S, D)),
        "qw": _w(qw),
        "scales": _w(torch.abs((lambda *s: torch.randn(*s, dtype=torch.bfloat16) * 0.05)(ng, F)) + 0.01),
        "zeros": _w(torch.full((ng, F), 8.0, dtype=float)),
    }

def reference(x, qw, scales, zeros):
    B, S, D = x.shape
    ng, F = scales.shape
    G = D // ng
    s = (lambda x, n, axis: torch.repeat_interleave(x, n, dim=axis))(scales, G, axis=0)
    z = (lambda x, n, axis: torch.repeat_interleave(x, n, dim=axis))(zeros, G, axis=0)
    w = ((lambda x, dt: x.to(dt))(qw, x.dtype) - z) * s
    return x @ w
```

```cpp
#include <hip/hip_runtime.h>
#include <hip/hip_bf16.h>

__device__ __forceinline__ float bf2f(unsigned short h) { return __uint_as_float(((unsigned)h) << 16); }

__global__ void naive_kernel(const unsigned short* x, const int* qw, const unsigned short* sc, const unsigned short* zr,
                             __hip_bfloat16* out, int M, int K, int N, int G) {
    int n = blockIdx.x * blockDim.x + threadIdx.x;
    int m = blockIdx.y;
    if (n >= N) return;
    float acc = 0.f;
    for (int k = 0; k < K; ++k) {
        int g = k / G;
        float s = bf2f(sc[g * N + n]), z = bf2f(zr[g * N + n]);
        float w = __bfloat162float(__float2bfloat16(((float)qw[k * N + n] - z) * s));
        acc += bf2f(x[m * K + k]) * w;
    }
    out[m * N + n] = __float2bfloat16(acc);
}

extern "C" void kernel_launch(void* const* d_in, const int* in_sizes, int n_in,
                              void* d_out, int out_size, void* d_ws, size_t ws_size,
                              hipStream_t stream) {
    const int K = 1024, N = 3584, M = 2048, G = 128;
    naive_kernel<<<dim3(N / 256, M), 256, 0, stream>>>((const unsigned short*)d_in[0], (const int*)d_in[1],
        (const unsigned short*)d_in[2], (const unsigned short*)d_in[3], (__hip_bfloat16*)d_out, M, K, N, G);
}
```

```cpp
#include <hip/hip_runtime.h>
#include <hip/hip_bf16.h>

typedef short bf16x8 __attribute__((ext_vector_type(8)));
typedef float f32x4 __attribute__((ext_vector_type(4)));
typedef unsigned u32x4 __attribute__((ext_vector_type(4)));
typedef unsigned u32x2 __attribute__((ext_vector_type(2)));
#define LAS __attribute__((address_space(3)))

namespace {
constexpr int M_ = 2048, N_ = 3584, K_ = 1024, G_ = 128, NG = K_ / G_;
constexpr int BM = 256, BN = 112, BK = 64, NIT = K_ / BK;
constexpr int A_STAGE = BM * BK * 2;
constexpr int B_STAGE = BN * BK * 2;
constexpr int LDS_A = 0, LDS_B = 4 * A_STAGE, LDS_SZ = LDS_B + 2 * B_STAGE;
constexpr int LDS_TOTAL = LDS_SZ + NG * BN * 4;
constexpr int QROW = N_ * 4;
constexpr int XROW = K_ * 2;

#define WAIT_V(n) asm volatile("s_waitcnt vmcnt(%0)" ::"n"(n) : "memory")
#define WAIT_L0() asm volatile("s_waitcnt lgkmcnt(0)" ::: "memory")
#define BAR() do { asm volatile("" ::: "memory"); __builtin_amdgcn_s_barrier(); asm volatile("" ::: "memory"); } while (0)

__device__ __forceinline__ unsigned cvt_pk_bf16(float lo, float hi) {
    unsigned r; asm("v_cvt_pk_bf16_f32 %0, %1, %2" : "=v"(r) : "v"(lo), "v"(hi)); return r;
}

struct Prod {
    __amdgpu_buffer_rsrc_t srdQ, srdX;
    unsigned vq;
    unsigned vx;
    unsigned m0a;
    unsigned wa0, wa1;
    unsigned sza;
    bool act;
    u32x4 q[4][8];
    float S[4], C[4];
};

template <int SLOT> __device__ __forceinline__ void issue_B(Prod& p, int b) {
    const unsigned so = (unsigned)b * (BK * QROW);
#pragma unroll
    for (int i = 0; i < 8; ++i)
        asm volatile("buffer_load_dwordx4 %0, %1, %2, %3 offen" : "=v"(p.q[SLOT][i]) : "v"(p.vq), "s"(p.srdQ), "s"(so + (unsigned)i * QROW) : "memory");
}
template <int SLOT> __device__ __forceinline__ void issue_A(Prod& p, int b) {
    const unsigned so = (unsigned)b * (BK * 2);
#pragma unroll
    for (int j = 0; j < 8; ++j)
        asm volatile("s_mov_b32 m0, %0\n\ts_nop 0\n\tbuffer_load_dwordx4 %1, %2, %3 offen lds"
                     :: "s"(p.m0a + (unsigned)(SLOT * A_STAGE + j * 4096)), "v"(p.vx), "s"(p.srdX), "s"(so + (unsigned)(j * 32 * XROW)) : "memory");
}
template <int SLOT, int W> __device__ __forceinline__ void wait_B(Prod& p) {
    asm volatile("s_waitcnt vmcnt(%8)"
                 : "+v"(p.q[SLOT][0]), "+v"(p.q[SLOT][1]), "+v"(p.q[SLOT][2]), "+v"(p.q[SLOT][3]),
                   "+v"(p.q[SLOT][4]), "+v"(p.q[SLOT][5]), "+v"(p.q[SLOT][6]), "+v"(p.q[SLOT][7])
                 : "n"(W) : "memory");
}
__device__ __forceinline__ void load_scales(Prod& p, LAS char* lds, int g) {
    const u32x4 sz = *(LAS const u32x4*)(lds + p.sza + g * (BN * 4));
#pragma unroll
    for (int j = 0; j < 4; ++j) {
        const float s = __uint_as_float(sz[j] << 16), z = __uint_as_float(sz[j] & 0xffff0000u);
        p.S[j] = s * 16777216.0f;
        p.C[j] = -z * s;
    }
}
template <int SLOT, int BUF> __device__ __forceinline__ void dequant_write(Prod& p, LAS char* lds) {
#pragma unroll
    for (int j = 0; j < 4; ++j) {
        u32x4 o; float t0, t1, t2, t3, t4, t5, t6, t7;
        asm("v_fma_mix_f32 %4, %12, %20, %21 op_sel_hi:[1,0,0]\n\t"
            "v_fma_mix_f32 %5, %13, %20, %21 op_sel_hi:[1,0,0]\n\t"
            "v_fma_mix_f32 %6, %14, %20, %21 op_sel_hi:[1,0,0]\n\t"
            "v_fma_mix_f32 %7, %15, %20, %21 op_sel_hi:[1,0,0]\n\t"
            "v_fma_mix_f32 %8, %16, %20, %21 op_sel_hi:[1,0,0]\n\t"
            "v_fma_mix_f32 %9, %17, %20, %21 op_sel_hi:[1,0,0]\n\t"
            "v_fma_mix_f32 %10, %18, %20, %21 op_sel_hi:[1,0,0]\n\t"
            "v_fma_mix_f32 %11, %19, %20, %21 op_sel_hi:[1,0,0]\n\t"
            "v_cvt_pk_bf16_f32 %0, %4, %5\n\t"
            "v_cvt_pk_bf16_f32 %1, %6, %7\n\t"
            "v_cvt_pk_bf16_f32 %2, %8, %9\n\t"
            "v_cvt_pk_bf16_f32 %3, %10, %11"
            : "=&v"(o[0]), "=&v"(o[1]), "=&v"(o[2]), "=&v"(o[3]),
              "=&v"(t0), "=&v"(t1), "=&v"(t2), "=&v"(t3), "=&v"(t4), "=&v"(t5), "=&v"(t6), "=&v"(t7)
            : "v"(p.q[SLOT][0][j]), "v"(p.q[SLOT][1][j]), "v"(p.q[SLOT][2][j]), "v"(p.q[SLOT][3][j]),
              "v"(p.q[SLOT][4][j]), "v"(p.q[SLOT][5][j]), "v"(p.q[SLOT][6][j]), "v"(p.q[SLOT][7][j]),
              "v"(p.S[j]), "v"(p.C[j]));
        if (p.act) *(LAS u32x4*)(lds + ((j >> 1) ? p.wa1 : p.wa0) + j * 128 + BUF * B_STAGE) = o;
    }
}
template <int R, bool DOA, bool DOB, int WA> __device__ __forceinline__ void pstep(Prod& p, LAS char* lds, int b) {
    if constexpr (DOA) issue_A<(R + 2) & 3>(p, b + 2);
    if ((R & 1) == 0) load_scales(p, lds, b >> 1);
    wait_B<R, 48>(p);
    dequant_write<R, R & 1>(p, lds);
    if constexpr (DOB) issue_B<R>(p, b + 4);
    WAIT_V(WA);
    WAIT_L0();
    BAR();
}

struct Frag { bf16x8 x[4]; bf16x8 w[7]; };
template <int SLOT, int KS> __device__ __forceinline__ void read_frags(Frag& f, LAS char* lds, unsigned xa, unsigned wa) {
#pragma unroll
    for (int t = 0; t < 7; ++t)
        f.w[t] = *(LAS const bf16x8*)(lds + ((wa ^ ((t & 1) * 16) ^ (KS * 64)) + t * 2048 + (SLOT & 1) * B_STAGE));
#pragma unroll
    for (int m = 0; m < 4; ++m)
        f.x[m] = *(LAS const bf16x8*)(lds + ((xa ^ (KS * 64)) + m * 2048 + SLOT * A_STAGE));
}
__device__ __forceinline__ void mma(f32x4 (&acc)[4][7], const Frag& f) {
#pragma unroll
    for (int m = 0; m < 4; ++m)
#pragma unroll
        for (int t = 0; t < 7; ++t)
            acc[m][t] = __builtin_amdgcn_mfma_f32_16x16x32_bf16(f.w[t], f.x[m], acc[m][t], 0, 0, 0);
}
template <int R, bool LAST> __device__ __forceinline__ void cstep(f32x4 (&acc)[4][7], Frag& f0, Frag& f1, LAS char* lds, unsigned xa, unsigned wa) {
    read_frags<R, 1>(f1, lds, xa, wa);
    __builtin_amdgcn_sched_barrier(0);
    mma(acc, f0);
    __builtin_amdgcn_sched_barrier(0);
    if constexpr (!LAST) {
        WAIT_L0();
        BAR();
        read_frags<(R + 1) & 3, 0>(f0, lds, xa, wa);
        __builtin_amdgcn_sched_barrier(0);
    }
    mma(acc, f1);
    __builtin_amdgcn_sched_barrier(0);
}

__global__ __launch_bounds__(512, 2)
void w4a16_kernel(const unsigned short* __restrict__ x, const int* __restrict__ qw, const unsigned short* __restrict__ sc,
                  const unsigned short* __restrict__ zr, unsigned short* __restrict__ out) {
    __shared__ __attribute__((aligned(1024))) char smem[LDS_TOTAL];
    LAS char* lds = (LAS char*)smem;
    const int lane = threadIdx.x & 63;
    const int wid = __builtin_amdgcn_readfirstlane(threadIdx.x >> 6);
    const int bid = blockIdx.x, xcd = bid & 7, idx = bid >> 3;
    const int tm = idx & 7, tn = xcd * 4 + (idx >> 3);
    const int m0 = tm * BM, n0 = tn * BN;

    if (wid >= 4) {
        const int pw = wid - 4;
        Prod p;
        p.srdQ = __builtin_amdgcn_make_buffer_rsrc((void*)qw, (short)0, K_ * N_ * 4, 0x00020000);
        p.srdX = __builtin_amdgcn_make_buffer_rsrc((void*)x, (short)0, M_ * K_ * 2, 0x00020000);
        const int kb = 2 * pw + (lane >> 5), nb = lane & 31;
        p.act = nb < 28;
        p.vq = p.act ? (unsigned)(kb * 8 * QROW + (n0 + 4 * nb) * 4) : 0x80000000u;
        const int rin = pw * 8 + (lane >> 3);
        p.vx = (unsigned)((m0 + rin) * XROW + (((lane & 7) ^ ((rin >> 1) & 7)) * 16));
        p.m0a = (unsigned)(uintptr_t)lds + LDS_A + pw * 1024;
        const int rb = nb < 24 ? (32 * (nb >> 3) + 16 * (nb & 1) + 4 * ((nb >> 1) & 3)) : (96 + 4 * (nb - 24));
        const int f = ((rb >> 1) & 7) ^ ((rb >> 4) & 1);
        p.wa0 = (unsigned)(LDS_B + rb * 128 + ((kb ^ f) * 16));
        p.wa1 = p.wa0 ^ 16u;
        p.sza = (unsigned)(LDS_SZ + (p.act ? nb : 0) * 16);
        issue_B<0>(p, 0); issue_A<0>(p, 0);
        issue_B<1>(p, 1); issue_A<1>(p, 1);
        issue_B<2>(p, 2); issue_B<3>(p, 3);
        BAR();
        for (int b = 0; b < 12; b += 4) {
            pstep<0, true, true, 40>(p, lds, b);
            pstep<1, true, true, 40>(p, lds, b + 1);
            pstep<2, true, true, 40>(p, lds, b + 2);
            pstep<3, true, true, 40>(p, lds, b + 3);
        }
        pstep<0, true, false, 32>(p, lds, 12);
        pstep<1, true, false, 24>(p, lds, 13);
        pstep<2, false, false, 8>(p, lds, 14);
        pstep<3, false, false, 0>(p, lds, 15);
    } else {
        const int w = wid;
        {
            const int t = threadIdx.x;
            if (t < NG * (BN / 4)) {
                const int g = t / (BN / 4), c4 = t % (BN / 4);
                const u32x2 s = *(const u32x2*)(sc + g * N_ + n0 + c4 * 4);
                const u32x2 z = *(const u32x2*)(zr + g * N_ + n0 + c4 * 4);
                u32x4 o;
                o[0] = (s[0] & 0xffffu) | (z[0] << 16);
                o[1] = (s[0] >> 16) | (z[0] & 0xffff0000u);
                o[2] = (s[1] & 0xffffu) | (z[1] << 16);
                o[3] = (s[1] >> 16) | (z[1] & 0xffff0000u);
                *(LAS u32x4*)(lds + LDS_SZ + t * 16) = o;
            }
            WAIT_L0();
            BAR();
        }
        const int fr = lane & 15, fq = lane >> 4;
        const unsigned xa = (unsigned)(LDS_A + (w * 64 + fr) * 128 + ((fq ^ (fr >> 1)) * 16));
        const unsigned wa = (unsigned)(LDS_B + fr * 128 + ((fq ^ (fr >> 1)) * 16));
        f32x4 acc[4][7];
#pragma unroll
        for (int m = 0; m < 4; ++m)
#pragma unroll
            for (int t = 0; t < 7; ++t) acc[m][t] = (f32x4){0.f, 0.f, 0.f, 0.f};
        Frag f0, f1;
        BAR();
        read_frags<0, 0>(f0, lds, xa, wa);
        for (int it = 0; it < 12; it += 4) {
            cstep<0, false>(acc, f0, f1, lds, xa, wa);
            cstep<1, false>(acc, f0, f1, lds, xa, wa);
            cstep<2, false>(acc, f0, f1, lds, xa, wa);
            cstep<3, false>(acc, f0, f1, lds, xa, wa);
        }
        cstep<0, false>(acc, f0, f1, lds, xa, wa);
        cstep<1, false>(acc, f0, f1, lds, xa, wa);
        cstep<2, false>(acc, f0, f1, lds, xa, wa);
        cstep<3, true>(acc, f0, f1, lds, xa, wa);
#pragma unroll
        for (int m = 0; m < 4; ++m) {
            unsigned short* orow = out + (size_t)(m0 + w * 64 + m * 16 + fr) * N_ + n0;
#pragma unroll
            for (int pp = 0; pp < 3; ++pp) {
                u32x4 v;
                v[0] = cvt_pk_bf16(acc[m][2 * pp][0], acc[m][2 * pp][1]);
                v[1] = cvt_pk_bf16(acc[m][2 * pp][2], acc[m][2 * pp][3]);
                v[2] = cvt_pk_bf16(acc[m][2 * pp + 1][0], acc[m][2 * pp + 1][1]);
                v[3] = cvt_pk_bf16(acc[m][2 * pp + 1][2], acc[m][2 * pp + 1][3]);
                *(u32x4*)(orow + 32 * pp + 8 * fq) = v;
            }
            u32x2 v;
            v[0] = cvt_pk_bf16(acc[m][6][0], acc[m][6][1]);
            v[1] = cvt_pk_bf16(acc[m][6][2], acc[m][6][3]);
            *(u32x2*)(orow + 96 + 4 * fq) = v;
        }
    }
}
}

extern "C" void kernel_launch(void* const* d_in, const int* in_sizes, int n_in,
                              void* d_out, int out_size, void* d_ws, size_t ws_size,
                              hipStream_t stream) {
    w4a16_kernel<<<256, 512, 0, stream>>>((const unsigned short*)d_in[0], (const int*)d_in[1],
        (const unsigned short*)d_in[2], (const unsigned short*)d_in[3], (unsigned short*)d_out);
}
```

```cpp
#include <hip/hip_runtime.h>
#include <hip/hip_bf16.h>

typedef short bf16x8 __attribute__((ext_vector_type(8)));
typedef float f32x4 __attribute__((ext_vector_type(4)));
typedef unsigned u32x4 __attribute__((ext_vector_type(4)));
typedef unsigned u32x2 __attribute__((ext_vector_type(2)));
#define LAS __attribute__((address_space(3)))

namespace {
constexpr int M_ = 2048, N_ = 3584, K_ = 1024, G_ = 128, NG = K_ / G_;
constexpr int BM = 256, BN = 112, BK = 64, NIT = K_ / BK;
constexpr int A_STAGE = BM * BK * 2;
constexpr int B_STAGE = BN * BK * 2;
constexpr int LDS_A = 0, LDS_B = 4 * A_STAGE, LDS_SZ = LDS_B + 2 * B_STAGE;
constexpr int LDS_TOTAL = LDS_SZ + NG * BN * 4;
constexpr int QROW = N_ * 4;
constexpr int XROW = K_ * 2;

#define STAMP(i)
#define STAMP_DECL
#define STAMP_ARG
#define STAMP_PASS
#define STAMP_OUT()
#define WAIT_V(n) asm volatile("s_waitcnt vmcnt(%0)" ::"n"(n) : "memory")
#define WAIT_L0() asm volatile("s_waitcnt lgkmcnt(0)" ::: "memory")
#define BAR() do { asm volatile("" ::: "memory"); __builtin_amdgcn_s_barrier(); asm volatile("" ::: "memory"); } while (0)

__device__ __forceinline__ unsigned cvt_pk_bf16(float lo, float hi) {
    unsigned r; asm("v_cvt_pk_bf16_f32 %0, %1, %2" : "=v"(r) : "v"(lo), "v"(hi)); return r;
}

struct Prod {
    __amdgpu_buffer_rsrc_t srdQ, srdX;
    unsigned vq;
    unsigned vx;
    unsigned m0a;
    unsigned wa0, wa1;
    unsigned sza;
    bool act;
    u32x4 q[4][8];
    float S[4], C[4];
};

template <int SLOT> __device__ __forceinline__ void issue_B(Prod& p, int b) {
    const unsigned so = (unsigned)b * (BK * QROW);
#pragma unroll
    for (int i = 0; i < 8; ++i)
        asm volatile("buffer_load_dwordx4 %0, %1, %2, %3 offen" : "=v"(p.q[SLOT][i]) : "v"(p.vq), "s"(p.srdQ), "s"(so + (unsigned)i * QROW) : "memory");
}
template <int SLOT> __device__ __forceinline__ void issue_A(Prod& p, int b) {
    const unsigned so = (unsigned)b * (BK * 2);
#pragma unroll
    for (int j = 0; j < 8; ++j)
        asm volatile("s_mov_b32 m0, %0\n\ts_nop 0\n\tbuffer_load_dwordx4 %1, %2, %3 offen lds"
                     :: "s"(p.m0a + (unsigned)(SLOT * A_STAGE + j * 4096)), "v"(p.vx), "s"(p.srdX), "s"(so + (unsigned)(j * 32 * XROW)) : "memory");
}
template <int SLOT, int W> __device__ __forceinline__ void wait_B(Prod& p) {
    asm volatile("s_waitcnt vmcnt(%8)"
                 : "+v"(p.q[SLOT][0]), "+v"(p.q[SLOT][1]), "+v"(p.q[SLOT][2]), "+v"(p.q[SLOT][3]),
                   "+v"(p.q[SLOT][4]), "+v"(p.q[SLOT][5]), "+v"(p.q[SLOT][6]), "+v"(p.q[SLOT][7])
                 : "n"(W) : "memory");
}
__device__ __forceinline__ void load_scales(Prod& p, LAS char* lds, int g) {
    const u32x4 sz = *(LAS const u32x4*)(lds + p.sza + g * (BN * 4));
#pragma unroll
    for (int j = 0; j < 4; ++j) {
        const float s = __uint_as_float(sz[j] << 16), z = __uint_as_float(sz[j] & 0xffff0000u);
        p.S[j] = s * 16777216.0f;
        p.C[j] = -z * s;
    }
}
template <int SLOT, int BUF> __device__ __forceinline__ void dequant_write(Prod& p, LAS char* lds) {
#pragma unroll
    for (int j = 0; j < 4; ++j) {
        u32x4 o; float t0, t1, t2, t3, t4, t5, t6, t7;
        asm("v_fma_mix_f32 %4, %12, %20, %21 op_sel_hi:[1,0,0]\n\t"
            "v_fma_mix_f32 %5, %13, %20, %21 op_sel_hi:[1,0,0]\n\t"
            "v_fma_mix_f32 %6, %14, %20, %21 op_sel_hi:[1,0,0]\n\t"
            "v_fma_mix_f32 %7, %15, %20, %21 op_sel_hi:[1,0,0]\n\t"
            "v_fma_mix_f32 %8, %16, %20, %21 op_sel_hi:[1,0,0]\n\t"
            "v_fma_mix_f32 %9, %17, %20, %21 op_sel_hi:[1,0,0]\n\t"
            "v_fma_mix_f32 %10, %18, %20, %21 op_sel_hi:[1,0,0]\n\t"
            "v_fma_mix_f32 %11, %19, %20, %21 op_sel_hi:[1,0,0]\n\t"
            "v_cvt_pk_bf16_f32 %0, %4, %5\n\t"
            "v_cvt_pk_bf16_f32 %1, %6, %7\n\t"
            "v_cvt_pk_bf16_f32 %2, %8, %9\n\t"
            "v_cvt_pk_bf16_f32 %3, %10, %11"
            : "=&v"(o[0]), "=&v"(o[1]), "=&v"(o[2]), "=&v"(o[3]),
              "=&v"(t0), "=&v"(t1), "=&v"(t2), "=&v"(t3), "=&v"(t4), "=&v"(t5), "=&v"(t6), "=&v"(t7)
            : "v"(p.q[SLOT][0][j]), "v"(p.q[SLOT][1][j]), "v"(p.q[SLOT][2][j]), "v"(p.q[SLOT][3][j]),
              "v"(p.q[SLOT][4][j]), "v"(p.q[SLOT][5][j]), "v"(p.q[SLOT][6][j]), "v"(p.q[SLOT][7][j]),
              "v"(p.S[j]), "v"(p.C[j]));
        if (p.act) *(LAS u32x4*)(lds + ((j >> 1) ? p.wa1 : p.wa0) + j * 128 + BUF * B_STAGE) = o;
    }
}
template <int R, bool DOA, bool DOB, int WA> __device__ __forceinline__ void pstep(Prod& p, LAS char* lds, int b STAMP_ARG) {
    if constexpr (DOA) issue_A<(R + 2) & 3>(p, b + 2);
    if ((R & 1) == 0) load_scales(p, lds, b >> 1);
    wait_B<R, 48>(p);
    dequant_write<R, R & 1>(p, lds);
    if constexpr (DOB) issue_B<R>(p, b + 4);
    WAIT_V(WA);
    WAIT_L0();
    BAR();
    STAMP(2 + b);
}

struct Frag { bf16x8 x[4]; bf16x8 w[7]; };
struct CAddr { unsigned x[2][2], w[2][2]; };
template <int SLOT, int KS> __device__ __forceinline__ void read_frags(Frag& f, const CAddr& ca) {
#pragma unroll
    for (int t = 0; t < 7; ++t)
        f.w[t] = *(LAS const bf16x8*)(uintptr_t)(ca.w[KS][t & 1] + (unsigned)(t * 2048 + (SLOT & 1) * B_STAGE));
#pragma unroll
    for (int m = 0; m < 4; ++m)
        f.x[m] = *(LAS const bf16x8*)(uintptr_t)(ca.x[SLOT >> 1][KS] + (unsigned)(m * 2048 + (SLOT & 1) * A_STAGE));
}
__device__ __forceinline__ void mma(f32x4 (&acc)[4][7], const Frag& f) {
#pragma unroll
    for (int m = 0; m < 4; ++m)
#pragma unroll
        for (int t = 0; t < 7; ++t)
            acc[m][t] = __builtin_amdgcn_mfma_f32_16x16x32_bf16(f.w[t], f.x[m], acc[m][t], 0, 0, 0);
}
#define PFD 0
struct Pf { __amdgpu_buffer_rsrc_t srdQ, srdX; unsigned pq, px, d; bool on; };
__device__ __forceinline__ void pf_step(Pf& pf, int s) {
    if (pf.on && s < NIT) {
        asm volatile("buffer_load_dword %0, %1, %2, %3 offen" : "+v"(pf.d) : "v"(pf.pq), "s"(pf.srdQ), "s"((unsigned)(s * BK * QROW)) : "memory");
        asm volatile("buffer_load_dword %0, %1, %2, %3 offen" : "+v"(pf.d) : "v"(pf.px), "s"(pf.srdX), "s"((unsigned)(s * BK * 2)) : "memory");
    }
}
template <int R, bool LAST> __device__ __forceinline__ void cstep(f32x4 (&acc)[4][7], Frag& f0, Frag& f1, const CAddr& ca, Pf& pf, int it STAMP_ARG) {
    pf_step(pf, it + PFD + 1);
    read_frags<R, 1>(f1, ca);
    __builtin_amdgcn_sched_barrier(0);
    mma(acc, f0);
    __builtin_amdgcn_sched_barrier(0);
    if constexpr (!LAST) {
        WAIT_L0();
        BAR();
        STAMP(3 + it);
        read_frags<(R + 1) & 3, 0>(f0, ca);
        __builtin_amdgcn_sched_barrier(0);
    }
    mma(acc, f1);
    __builtin_amdgcn_sched_barrier(0);
}

__global__ __launch_bounds__(512, 2)
void w4a16_kernel(const unsigned short* __restrict__ x, const int* __restrict__ qw, const unsigned short* __restrict__ sc,
                  const unsigned short* __restrict__ zr, unsigned short* __restrict__ out, void* ws) {
    __shared__ __attribute__((aligned(1024))) char smem[LDS_TOTAL];
    LAS char* lds = (LAS char*)smem;
    const int lane = threadIdx.x & 63;
    const int wid = __builtin_amdgcn_readfirstlane(threadIdx.x >> 6);
    const int bid = blockIdx.x, xcd = bid & 7, idx = bid >> 3;
    const int tm = idx & 7, tn = xcd * 4 + (idx >> 3);
    const int m0 = tm * BM, n0 = tn * BN;
    STAMP_DECL;
    STAMP(0);

    if (wid >= 4) {
        const int pw = wid - 4;
        Prod p;
        p.srdQ = __builtin_amdgcn_make_buffer_rsrc((void*)qw, (short)0, K_ * N_ * 4, 0x00020000);
        p.srdX = __builtin_amdgcn_make_buffer_rsrc((void*)x, (short)0, M_ * K_ * 2, 0x00020000);
        const int kb = 2 * pw + (lane >> 5), nb = lane & 31;
        p.act = nb < 28;
        p.vq = p.act ? (unsigned)(kb * 8 * QROW + (n0 + 4 * nb) * 4) : 0x80000000u;
        const int rin = pw * 8 + (lane >> 3);
        p.vx = (unsigned)((m0 + rin) * XROW + (((lane & 7) ^ ((rin >> 1) & 7)) * 16));
        p.m0a = (unsigned)(uintptr_t)lds + LDS_A + pw * 1024;
        const int rb = nb < 24 ? (32 * (nb >> 3) + 16 * (nb & 1) + 4 * ((nb >> 1) & 3)) : (96 + 4 * (nb - 24));
        const int f = ((rb >> 1) & 7) ^ ((rb >> 4) & 1);
        p.wa0 = (unsigned)(LDS_B + rb * 128 + ((kb ^ f) * 16));
        p.wa1 = p.wa0 ^ 16u;
        p.sza = (unsigned)(LDS_SZ + (p.act ? nb : 0) * 16);
        issue_B<0>(p, 0); issue_A<0>(p, 0);
        issue_B<1>(p, 1); issue_A<1>(p, 1);
        issue_B<2>(p, 2); issue_B<3>(p, 3);
        BAR();
        STAMP(1);
        for (int b = 0; b < 12; b += 4) {
            pstep<0, true, true, 40>(p, lds, b STAMP_PASS);
            pstep<1, true, true, 40>(p, lds, b + 1 STAMP_PASS);
            pstep<2, true, true, 40>(p, lds, b + 2 STAMP_PASS);
            pstep<3, true, true, 40>(p, lds, b + 3 STAMP_PASS);
        }
        pstep<0, true, false, 32>(p, lds, 12 STAMP_PASS);
        pstep<1, true, false, 24>(p, lds, 13 STAMP_PASS);
        pstep<2, false, false, 8>(p, lds, 14 STAMP_PASS);
        pstep<3, false, false, 0>(p, lds, 15 STAMP_PASS);
    } else {
        const int w = wid;
        Pf pf;
        pf.srdQ = __builtin_amdgcn_make_buffer_rsrc((void*)qw, (short)0, K_ * N_ * 4, 0x00020000);
        pf.srdX = __builtin_amdgcn_make_buffer_rsrc((void*)x, (short)0, M_ * K_ * 2, 0x00020000);
        pf.px = (unsigned)((idx * 64 + lane) * XROW);
        pf.pq = lane < 28 ? (unsigned)((2 * idx + lane / 14) * QROW + xcd * 1792 + (lane % 14) * 128) : 0x80000000u;
        pf.d = 0; pf.on = (w == 3) && (PFD > 0);
        {
            const int t = threadIdx.x;
            if (t < NG * (BN / 4)) {
                const int g = t / (BN / 4), c4 = t % (BN / 4);
                const u32x2 s = *(const u32x2*)(sc + g * N_ + n0 + c4 * 4);
                const u32x2 z = *(const u32x2*)(zr + g * N_ + n0 + c4 * 4);
                u32x4 o;
                o[0] = (s[0] & 0xffffu) | (z[0] << 16);
                o[1] = (s[0] >> 16) | (z[0] & 0xffff0000u);
                o[2] = (s[1] & 0xffffu) | (z[1] << 16);
                o[3] = (s[1] >> 16) | (z[1] & 0xffff0000u);
                *(LAS u32x4*)(lds + LDS_SZ + t * 16) = o;
            }
            for (int s = 1; s <= PFD; ++s) pf_step(pf, s);
            WAIT_L0();
            BAR();
            STAMP(1);
        }
        const int fr = lane & 15, fq = lane >> 4;
        const unsigned lbase = (unsigned)(uintptr_t)lds;
        const unsigned xa = (unsigned)(LDS_A + (w * 64 + fr) * 128 + ((fq ^ (fr >> 1)) * 16));
        const unsigned wa = (unsigned)(LDS_B + fr * 128 + ((fq ^ (fr >> 1)) * 16));
        CAddr ca;
#pragma unroll
        for (int a = 0; a < 2; ++a)
#pragma unroll
            for (int b = 0; b < 2; ++b) {
                ca.x[a][b] = lbase + (xa ^ (b * 64)) + a * 2 * A_STAGE;
                ca.w[a][b] = lbase + (wa ^ (a * 64) ^ (b * 16));
                asm volatile("" : "+v"(ca.x[a][b]), "+v"(ca.w[a][b]));
            }
        f32x4 acc[4][7];
#pragma unroll
        for (int m = 0; m < 4; ++m)
#pragma unroll
            for (int t = 0; t < 7; ++t) acc[m][t] = (f32x4){0.f, 0.f, 0.f, 0.f};
        Frag f0, f1;
        BAR();
        STAMP(2);
        read_frags<0, 0>(f0, ca);
        for (int it = 0; it < 12; it += 4) {
            cstep<0, false>(acc, f0, f1, ca, pf, it STAMP_PASS);
            cstep<1, false>(acc, f0, f1, ca, pf, it + 1 STAMP_PASS);
            cstep<2, false>(acc, f0, f1, ca, pf, it + 2 STAMP_PASS);
            cstep<3, false>(acc, f0, f1, ca, pf, it + 3 STAMP_PASS);
        }
        cstep<0, false>(acc, f0, f1, ca, pf, 12 STAMP_PASS);
        cstep<1, false>(acc, f0, f1, ca, pf, 13 STAMP_PASS);
        cstep<2, false>(acc, f0, f1, ca, pf, 14 STAMP_PASS);
        cstep<3, true>(acc, f0, f1, ca, pf, 15 STAMP_PASS);
        asm volatile("s_waitcnt vmcnt(0)" : "+v"(pf.d) :: "memory");
        STAMP(18);
#pragma unroll
        for (int m = 0; m < 4; ++m) {
            unsigned short* orow = out + (size_t)(m0 + w * 64 + m * 16 + fr) * N_ + n0;
#pragma unroll
            for (int pp = 0; pp < 3; ++pp) {
                u32x4 v;
                v[0] = cvt_pk_bf16(acc[m][2 * pp][0], acc[m][2 * pp][1]);
                v[1] = cvt_pk_bf16(acc[m][2 * pp][2], acc[m][2 * pp][3]);
                v[2] = cvt_pk_bf16(acc[m][2 * pp + 1][0], acc[m][2 * pp + 1][1]);
                v[3] = cvt_pk_bf16(acc[m][2 * pp + 1][2], acc[m][2 * pp + 1][3]);
                *(u32x4*)(orow + 32 * pp + 8 * fq) = v;
            }
            u32x2 v;
            v[0] = cvt_pk_bf16(acc[m][6][0], acc[m][6][1]);
            v[1] = cvt_pk_bf16(acc[m][6][2], acc[m][6][3]);
            *(u32x2*)(orow + 96 + 4 * fq) = v;
        }
        STAMP(19);
        WAIT_V(0);
        STAMP(20);
    }
    STAMP_OUT();
}
}

extern "C" void kernel_launch(void* const* d_in, const int* in_sizes, int n_in,
                              void* d_out, int out_size, void* d_ws, size_t ws_size,
                              hipStream_t stream) {
    w4a16_kernel<<<256, 512, 0, stream>>>((const unsigned short*)d_in[0], (const int*)d_in[1],
        (const unsigned short*)d_in[2], (const unsigned short*)d_in[3], (unsigned short*)d_out, d_ws);
}
```

```cpp
#include <hip/hip_runtime.h>
#include <hip/hip_bf16.h>

typedef short bf16x8 __attribute__((ext_vector_type(8)));
typedef float f32x4 __attribute__((ext_vector_type(4)));
typedef unsigned u32x4 __attribute__((ext_vector_type(4)));
typedef unsigned u32x2 __attribute__((ext_vector_type(2)));
#define LAS __attribute__((address_space(3)))
#define DI __device__ __forceinline__

#define PFD 4

namespace {
constexpr int M_ = 2048, N_ = 3584, K_ = 1024, G_ = 128, NG = K_ / G_;
constexpr int BM = 256, BN = 112, BK = 64, NIT = K_ / BK;
constexpr int A_STAGE = BM * BK * 2;
constexpr int B_STAGE = BN * BK * 2;
constexpr int LDS_A = 0, LDS_B = 4 * A_STAGE, LDS_SZ = LDS_B + 2 * B_STAGE;
constexpr int LDS_TOTAL = LDS_SZ + NG * BN * 4;
constexpr int QROW = N_ * 4;
constexpr int XROW = K_ * 2;

#define STAMP(i)
#define STAMP_DECL
#define STAMP_OUT()
#define WAIT_V(n) asm volatile("s_waitcnt vmcnt(%0)" ::"n"(n) : "memory")
#define WAIT_L0() asm volatile("s_waitcnt lgkmcnt(0)" ::: "memory")
#define BAR() do { asm volatile("" ::: "memory"); __builtin_amdgcn_s_barrier(); asm volatile("" ::: "memory"); } while (0)
#define SB0() __builtin_amdgcn_sched_barrier(0)

DI unsigned cvt_pk_bf16(float lo, float hi) { unsigned r; asm("v_cvt_pk_bf16_f32 %0, %1, %2" : "=v"(r) : "v"(lo), "v"(hi)); return r; }

DI void issue_B(u32x4 (&q)[8], unsigned vq, __amdgpu_buffer_rsrc_t srdQ, int b) {
    const unsigned so = (unsigned)b * (BK * QROW);
#pragma unroll
    for (int i = 0; i < 8; ++i)
        asm volatile("buffer_load_dwordx4 %0, %1, %2, %3 offen" : "=v"(q[i]) : "v"(vq), "s"(srdQ), "s"(so + (unsigned)i * QROW) : "memory");
}
DI void issue_A(unsigned m0a, unsigned vx, __amdgpu_buffer_rsrc_t srdX, int b) {
    const unsigned so = (unsigned)b * (BK * 2), mb = m0a + (unsigned)(b & 3) * A_STAGE;
#pragma unroll
    for (int j = 0; j < 8; ++j)
        asm volatile("s_mov_b32 m0, %0\n\ts_nop 0\n\tbuffer_load_dwordx4 %1, %2, %3 offen lds"
                     :: "s"(mb + (unsigned)(j * 4096)), "v"(vx), "s"(srdX), "s"(so + (unsigned)(j * 32 * XROW)) : "memory");
}
template <int W> DI void wait_B(u32x4 (&q)[8]) {
    asm volatile("s_waitcnt vmcnt(%8)"
                 : "+v"(q[0]), "+v"(q[1]), "+v"(q[2]), "+v"(q[3]), "+v"(q[4]), "+v"(q[5]), "+v"(q[6]), "+v"(q[7]) : "n"(W) : "memory");
}
DI void dequant_write(const u32x4 (&q)[8], const float (&S)[4], const float (&C)[4], unsigned wa0, unsigned wa1, int bufoff, bool act) {
#pragma unroll
    for (int j = 0; j < 4; ++j) {
        u32x4 o; float t0, t1, t2, t3, t4, t5, t6, t7;
        asm("v_fma_mix_f32 %4, %12, %20, %21 op_sel_hi:[1,0,0]\n\t"
            "v_fma_mix_f32 %5, %13, %20, %21 op_sel_hi:[1,0,0]\n\t"
            "v_fma_mix_f32 %6, %14, %20, %21 op_sel_hi:[1,0,0]\n\t"
            "v_fma_mix_f32 %7, %15, %20, %21 op_sel_hi:[1,0,0]\n\t"
            "v_fma_mix_f32 %8, %16, %20, %21 op_sel_hi:[1,0,0]\n\t"
            "v_fma_mix_f32 %9, %17, %20, %21 op_sel_hi:[1,0,0]\n\t"
            "v_fma_mix_f32 %10, %18, %20, %21 op_sel_hi:[1,0,0]\n\t"
            "v_fma_mix_f32 %11, %19, %20, %21 op_sel_hi:[1,0,0]\n\t"
            "v_cvt_pk_bf16_f32 %0, %4, %5\n\t"
            "v_cvt_pk_bf16_f32 %1, %6, %7\n\t"
            "v_cvt_pk_bf16_f32 %2, %8, %9\n\t"
            "v_cvt_pk_bf16_f32 %3, %10, %11"
            : "=&v"(o[0]), "=&v"(o[1]), "=&v"(o[2]), "=&v"(o[3]),
              "=&v"(t0), "=&v"(t1), "=&v"(t2), "=&v"(t3), "=&v"(t4), "=&v"(t5), "=&v"(t6), "=&v"(t7)
            : "v"(q[0][j]), "v"(q[1][j]), "v"(q[2][j]), "v"(q[3][j]), "v"(q[4][j]), "v"(q[5][j]), "v"(q[6][j]), "v"(q[7][j]),
              "v"(S[j]), "v"(C[j]));
        if (act) *(LAS u32x4*)(uintptr_t)(((j >> 1) ? wa1 : wa0) + (unsigned)(j * 128 + bufoff)) = o;
    }
}

struct Frag { bf16x8 x[4]; bf16x8 w[7]; };
DI void read_frags(Frag& f, unsigned xa, unsigned w0, unsigned w1, int bufoff) {
    f.x[0] = *(LAS const bf16x8*)(uintptr_t)(xa);
#pragma unroll
    for (int t = 0; t < 7; ++t)
        f.w[t] = *(LAS const bf16x8*)(uintptr_t)(((t & 1) ? w1 : w0) + (unsigned)(t * 2048 + bufoff));
#pragma unroll
    for (int m = 1; m < 4; ++m)
        f.x[m] = *(LAS const bf16x8*)(uintptr_t)(xa + (unsigned)(m * 2048));
}
DI void mma(f32x4 (&acc)[4][7], const Frag& f) {
#pragma unroll
    for (int m = 0; m < 4; ++m)
#pragma unroll
        for (int t = 0; t < 7; ++t)
            acc[m][t] = __builtin_amdgcn_mfma_f32_16x16x32_bf16(f.w[t], f.x[m], acc[m][t], 0, 0, 0);
}
DI void interleave() {
#pragma unroll
    for (int i = 0; i < 11; ++i) {
        __builtin_amdgcn_sched_group_barrier(0x008, 2, 0);
        __builtin_amdgcn_sched_group_barrier(0x100, 1, 0);
    }
    __builtin_amdgcn_sched_group_barrier(0x008, 6, 0);
}

__global__ __launch_bounds__(512, 2)
void w4a16_kernel(const unsigned short* __restrict__ x, const int* __restrict__ qw, const unsigned short* __restrict__ sc,
                  const unsigned short* __restrict__ zr, unsigned short* __restrict__ out, void* ws) {
    __shared__ __attribute__((aligned(1024))) char smem[LDS_TOTAL];
    const unsigned lbase = (unsigned)(uintptr_t)(LAS char*)smem;
    const int lane = threadIdx.x & 63;
    const int wid = __builtin_amdgcn_readfirstlane(threadIdx.x >> 6);
    const int bid = blockIdx.x, xcd = bid & 7, idx = bid >> 3;
    const int tm = idx & 7, tn = xcd * 4 + (idx >> 3);
    const int m0 = tm * BM, n0 = tn * BN;
    STAMP_DECL;
    STAMP(0);

    if (wid >= 4) {
        const int pw = wid - 4;
        const __amdgpu_buffer_rsrc_t srdQ = __builtin_amdgcn_make_buffer_rsrc((void*)qw, (short)0, K_ * N_ * 4, 0x00020000);
        const __amdgpu_buffer_rsrc_t srdX = __builtin_amdgcn_make_buffer_rsrc((void*)x, (short)0, M_ * K_ * 2, 0x00020000);
        const int kb = 2 * pw + (lane >> 5), nb = lane & 31;
        const bool act = nb < 28;
        const unsigned vq = act ? (unsigned)(kb * 8 * QROW + (n0 + 4 * nb) * 4) : 0x80000000u;
        const int rin = pw * 8 + (lane >> 3);
        const unsigned vx = (unsigned)((m0 + rin) * XROW + (((lane & 7) ^ ((rin >> 1) & 7)) * 16));
        const unsigned m0a = lbase + LDS_A + pw * 1024;
        u32x4 q0[8], q1[8];
        unsigned pfd = 0;
        constexpr unsigned OFF = 0x40000000u;
        const unsigned pq = lane < 56 ? (unsigned)((lane / 14) * QROW + xcd * 1792 + (lane % 14) * 128) : 0x80000000u;
        const unsigned pxg = (unsigned)((bid * 8 + (lane & 7)) * XROW + (lane >> 3) * 128);
#define PF_Q(so) asm volatile("buffer_load_dword %0, %1, %2, %3 offen" : "+v"(pfd) : "v"(pq), "s"(srdQ), "s"(so) : "memory")
#define PF_X(so) asm volatile("buffer_load_dword %0, %1, %2, %3 offen" : "+v"(pfd) : "v"(pxg), "s"(srdX), "s"(so) : "memory")
        issue_B(q0, vq, srdQ, 0); issue_A(m0a, vx, srdX, 0);
        PF_Q((PFD && pw < 2) ? (unsigned)((pw * 128 + 64 + idx * 4) * QROW) : OFF);
        issue_B(q1, vq, srdQ, 1); issue_A(m0a, vx, srdX, 1);
        PF_X((PFD && pw < 2) ? (unsigned)(pw * 1024) : OFF);
        const int rb = nb < 24 ? (32 * (nb >> 3) + 16 * (nb & 1) + 4 * ((nb >> 1) & 3)) : (96 + 4 * (nb - 24));
        const int f = ((rb >> 1) & 7) ^ ((rb >> 4) & 1);
        const unsigned wa0 = lbase + (unsigned)(LDS_B + rb * 128 + ((kb ^ f) * 16));
        const unsigned wa1 = wa0 ^ 16u;
        unsigned sza = lbase + (unsigned)(LDS_SZ + (act ? nb : 0) * 16);
        BAR();
        STAMP(1);
        float S[4], C[4];
#define LOAD_SCALES() do { const u32x4 sz = *(LAS const u32x4*)(uintptr_t)sza; sza += BN * 4; \
            _Pragma("unroll") for (int j = 0; j < 4; ++j) { \
                const float s = __uint_as_float(sz[j] << 16), z = __uint_as_float(sz[j] & 0xffff0000u); \
                S[j] = s * 16777216.0f; C[j] = -z * s; } } while (0)
#define PF_TURN(b) PF_Q((PFD && (b) + 5 < NIT && ((b) & 7) == (idx & 7)) ? (unsigned)((((b) + 5) * 64 + (idx >> 3) * 16 + pw * 4) * QROW) : OFF)
        for (int b = 0; b < NIT - 2; b += 2) {
            LOAD_SCALES();
            wait_B<26>(q0);
            dequant_write(q0, S, C, wa0, wa1, 0, act);
            WAIT_V(18);
            WAIT_L0();
            BAR();
            STAMP(2 + b);
            issue_B(q0, vq, srdQ, b + 2); issue_A(m0a, vx, srdX, b + 2);
            PF_TURN(b);
            wait_B<26>(q1);
            dequant_write(q1, S, C, wa0, wa1, B_STAGE, act);
            WAIT_V(18);
            WAIT_L0();
            BAR();
            STAMP(3 + b);
            issue_B(q1, vq, srdQ, b + 3); issue_A(m0a, vx, srdX, b + 3);
            PF_TURN(b + 1);
        }
        LOAD_SCALES();
        wait_B<26>(q0);
        dequant_write(q0, S, C, wa0, wa1, 0, act);
        WAIT_V(18);
        WAIT_L0();
        BAR();
        STAMP(16);
        wait_B<9>(q1);
        dequant_write(q1, S, C, wa0, wa1, B_STAGE, act);
        asm volatile("s_waitcnt vmcnt(0)" : "+v"(pfd) :: "memory");
        WAIT_L0();
        BAR();
        STAMP(17);
        BAR();
    } else {
        const int w = wid;
        {
            const int t = threadIdx.x;
            if (t < NG * (BN / 4)) {
                const int g = t / (BN / 4), c4 = t % (BN / 4);
                const u32x2 s = *(const u32x2*)(sc + g * N_ + n0 + c4 * 4);
                const u32x2 z = *(const u32x2*)(zr + g * N_ + n0 + c4 * 4);
                u32x4 o;
                o[0] = (s[0] & 0xffffu) | (z[0] << 16);
                o[1] = (s[0] >> 16) | (z[0] & 0xffff0000u);
                o[2] = (s[1] & 0xffffu) | (z[1] << 16);
                o[3] = (s[1] >> 16) | (z[1] & 0xffff0000u);
                *(LAS u32x4*)(uintptr_t)(lbase + LDS_SZ + t * 16) = o;
            }
            __builtin_amdgcn_s_waitcnt(0xC07F);
            BAR();
            STAMP(1);
        }
        const int fr = lane & 15, fq = lane >> 4;
        const unsigned sw = (unsigned)((fq ^ (fr >> 1)) * 16);
        unsigned xa0 = lbase + (unsigned)(LDS_A + (w * 64 + fr) * 128) + sw;
        unsigned w00 = lbase + (unsigned)(LDS_B + fr * 128) + sw;
        unsigned w01 = w00 ^ 16u, w10 = w00 ^ 64u, w11 = w00 ^ 80u;
        asm volatile("" : "+v"(w00), "+v"(w01), "+v"(w10), "+v"(w11));
        f32x4 acc[4][7];
#pragma unroll
        for (int m = 0; m < 4; ++m)
#pragma unroll
            for (int t = 0; t < 7; ++t) acc[m][t] = (f32x4){0.f, 0.f, 0.f, 0.f};
        Frag f0, f1;
        BAR();
        STAMP(2);
        read_frags(f0, xa0, w00, w01, 0);
        for (int it = 0; it < NIT; it += 2) {
            unsigned xe = xa0 + (unsigned)((it & 2) * A_STAGE);
            unsigned xn = xa0 + (unsigned)(((it + 2) & 2) * A_STAGE);
            asm volatile("" : "+v"(xe), "+v"(xn));
            SB0();
            read_frags(f1, xe ^ 64u, w10, w11, 0);
            mma(acc, f0);
            interleave();
            SB0();
            WAIT_L0();
            BAR();
            STAMP(3 + it);
            SB0();
            read_frags(f0, xe + A_STAGE, w00, w01, B_STAGE);
            mma(acc, f1);
            interleave();
            SB0();
            read_frags(f1, (xe ^ 64u) + A_STAGE, w10, w11, B_STAGE);
            mma(acc, f0);
            interleave();
            SB0();
            WAIT_L0();
            BAR();
            STAMP(4 + it);
            SB0();
            read_frags(f0, xn, w00, w01, 0);
            mma(acc, f1);
            interleave();
            SB0();
        }
        STAMP(19);
#pragma unroll
        for (int m = 0; m < 4; ++m) {
            unsigned short* orow = out + (size_t)(m0 + w * 64 + m * 16 + fr) * N_ + n0;
#pragma unroll
            for (int pp = 0; pp < 3; ++pp) {
                u32x4 v;
                v[0] = cvt_pk_bf16(acc[m][2 * pp][0], acc[m][2 * pp][1]);
                v[1] = cvt_pk_bf16(acc[m][2 * pp][2], acc[m][2 * pp][3]);
                v[2] = cvt_pk_bf16(acc[m][2 * pp + 1][0], acc[m][2 * pp + 1][1]);
                v[3] = cvt_pk_bf16(acc[m][2 * pp + 1][2], acc[m][2 * pp + 1][3]);
                *(u32x4*)(orow + 32 * pp + 8 * fq) = v;
            }
            u32x2 v;
            v[0] = cvt_pk_bf16(acc[m][6][0], acc[m][6][1]);
            v[1] = cvt_pk_bf16(acc[m][6][2], acc[m][6][3]);
            *(u32x2*)(orow + 96 + 4 * fq) = v;
        }
        STAMP(20);
        STAMP(21);
    }
    STAMP_OUT();
}
}

extern "C" void kernel_launch(void* const* d_in, const int* in_sizes, int n_in,
                              void* d_out, int out_size, void* d_ws, size_t ws_size,
                              hipStream_t stream) {
    w4a16_kernel<<<256, 512, 0, stream>>>((const unsigned short*)d_in[0], (const int*)d_in[1],
        (const unsigned short*)d_in[2], (const unsigned short*)d_in[3], (unsigned short*)d_out, d_ws);
}
```

```cpp
#include <hip/hip_runtime.h>
#include <hip/hip_bf16.h>

typedef short bf16x8 __attribute__((ext_vector_type(8)));
typedef float f32x4 __attribute__((ext_vector_type(4)));
typedef unsigned u32x4 __attribute__((ext_vector_type(4)));
typedef unsigned u32x2 __attribute__((ext_vector_type(2)));
#define LAS __attribute__((address_space(3)))
#define DI __device__ __forceinline__

#define PFD 1
#define EPI 1

namespace {
constexpr int M_ = 2048, N_ = 3584, K_ = 1024, G_ = 128, NG = K_ / G_;
constexpr int BM = 256, BN = 112, BK = 64, NIT = K_ / BK;
constexpr int A_STAGE = BM * BK * 2;
constexpr int B_STAGE = BN * BK * 2;
constexpr int LDS_A = 0, LDS_B = 4 * A_STAGE, LDS_SZ = LDS_B + 2 * B_STAGE;
constexpr int LDS_PF = LDS_SZ + NG * BN * 4;
constexpr int LDS_TOTAL = LDS_PF + 256;
constexpr int QROW = N_ * 4;
constexpr int XROW = K_ * 2;
constexpr int OROW = 240;

#define STAMP(i)
#define STAMP_DECL
#define STAMP_OUT()
#define LSTAMP(i) STAMP(i)
#define WAIT_V(n) asm volatile("s_waitcnt vmcnt(%0)" ::"n"(n) : "memory")
#define WAIT_L0() asm volatile("s_waitcnt lgkmcnt(0)" ::: "memory")
#define BAR() do { asm volatile("" ::: "memory"); __builtin_amdgcn_s_barrier(); asm volatile("" ::: "memory"); } while (0)
#define SB0() __builtin_amdgcn_sched_barrier(0)

DI unsigned cvt_pk_bf16(float lo, float hi) { unsigned r; asm("v_cvt_pk_bf16_f32 %0, %1, %2" : "=v"(r) : "v"(lo), "v"(hi)); return r; }

DI float fmul(float a, float b) { float r; asm volatile("v_mul_f32 %0, %1, %2" : "=v"(r) : "v"(a), "v"(b)); return r; }
DI void ld_q(u32x4& q, unsigned vq, __amdgpu_buffer_rsrc_t srd, unsigned so) {
    asm volatile("buffer_load_dwordx4 %0, %1, %2, %3 offen" : "=v"(q) : "v"(vq), "s"(srd), "s"(so) : "memory");
}
DI void dma16(unsigned m0v, unsigned v, __amdgpu_buffer_rsrc_t srd, unsigned so) {
    asm volatile("s_mov_b32 m0, %0\n\ts_nop 0\n\tbuffer_load_dwordx4 %1, %2, %3 offen lds" :: "s"(m0v), "v"(v), "s"(srd), "s"(so) : "memory");
}
DI void dma4(unsigned m0v, unsigned v, __amdgpu_buffer_rsrc_t srd, unsigned so) {
    asm volatile("s_mov_b32 m0, %0\n\ts_nop 0\n\tbuffer_load_dword %1, %2, %3 offen lds" :: "s"(m0v), "v"(v), "s"(srd), "s"(so) : "memory");
}
template <int W> DI void wait_B(u32x4 (&q)[8]) {
    asm volatile("s_waitcnt vmcnt(%8)"
                 : "+v"(q[0]), "+v"(q[1]), "+v"(q[2]), "+v"(q[3]), "+v"(q[4]), "+v"(q[5]), "+v"(q[6]), "+v"(q[7]) : "n"(W) : "memory");
}
DI void dequant_pair(u32x4 (&o)[4], int kk, const u32x4& qa, const u32x4& qb, const float (&S)[4], const float (&C)[4]) {
    float t0, t1, t2, t3, t4, t5, t6, t7;
    asm volatile(
        "v_fma_mix_f32 %4, %12, %20, %24 op_sel_hi:[1,0,0]\n\t"
        "v_fma_mix_f32 %5, %16, %20, %24 op_sel_hi:[1,0,0]\n\t"
        "v_fma_mix_f32 %6, %13, %21, %25 op_sel_hi:[1,0,0]\n\t"
        "v_fma_mix_f32 %7, %17, %21, %25 op_sel_hi:[1,0,0]\n\t"
        "v_fma_mix_f32 %8, %14, %22, %26 op_sel_hi:[1,0,0]\n\t"
        "v_fma_mix_f32 %9, %18, %22, %26 op_sel_hi:[1,0,0]\n\t"
        "v_fma_mix_f32 %10, %15, %23, %27 op_sel_hi:[1,0,0]\n\t"
        "v_fma_mix_f32 %11, %19, %23, %27 op_sel_hi:[1,0,0]\n\t"
        "v_cvt_pk_bf16_f32 %0, %4, %5\n\t"
        "v_cvt_pk_bf16_f32 %1, %6, %7\n\t"
        "v_cvt_pk_bf16_f32 %2, %8, %9\n\t"
        "v_cvt_pk_bf16_f32 %3, %10, %11"
        : "=&v"(o[0][kk]), "=&v"(o[1][kk]), "=&v"(o[2][kk]), "=&v"(o[3][kk]),
          "=&v"(t0), "=&v"(t1), "=&v"(t2), "=&v"(t3), "=&v"(t4), "=&v"(t5), "=&v"(t6), "=&v"(t7)
        : "v"(qa[0]), "v"(qa[1]), "v"(qa[2]), "v"(qa[3]), "v"(qb[0]), "v"(qb[1]), "v"(qb[2]), "v"(qb[3]),
          "v"(S[0]), "v"(S[1]), "v"(S[2]), "v"(S[3]), "v"(C[0]), "v"(C[1]), "v"(C[2]), "v"(C[3]));
}
struct PCtx { unsigned vq, vx, m0a, wa0, wa1; __amdgpu_buffer_rsrc_t srdQ, srdX; bool act; };
template <bool ISSUE, int WV>
DI void pstep(u32x4 (&q)[8], const float (&S)[4], const float (&C)[4], const PCtx& c, int bufoff, int ns) {
    u32x4 o[4];
    const unsigned sq = (unsigned)ns * (BK * QROW), sx = (unsigned)ns * (BK * 2), mb = c.m0a + (unsigned)(ns & 3) * A_STAGE;
    if constexpr (ISSUE) {
#pragma unroll
        for (int j = 0; j < 4; ++j) dma16(mb + (unsigned)(j * 4096), c.vx, c.srdX, sx + (unsigned)(j * 32 * XROW));
    }
    wait_B<WV>(q);
#pragma unroll
    for (int kk = 0; kk < 4; ++kk) {
        dequant_pair(o, kk, q[2 * kk], q[2 * kk + 1], S, C);
        if (kk == 3 && c.act) {
#pragma unroll
            for (int j = 0; j < 4; ++j) *(LAS u32x4*)(uintptr_t)(((j >> 1) ? c.wa1 : c.wa0) + (unsigned)(j * 128 + bufoff)) = o[j];
        }
        if constexpr (ISSUE) {
            ld_q(q[2 * kk], c.vq, c.srdQ, sq + (unsigned)(2 * kk) * QROW);
            ld_q(q[2 * kk + 1], c.vq, c.srdQ, sq + (unsigned)(2 * kk + 1) * QROW);
        }
    }
    if constexpr (ISSUE) {
#pragma unroll
        for (int j = 4; j < 8; ++j) dma16(mb + (unsigned)(j * 4096), c.vx, c.srdX, sx + (unsigned)(j * 32 * XROW));
    }
}

struct Frag { bf16x8 x[4]; bf16x8 w[7]; };
DI void read_frags(Frag& f, unsigned xa, unsigned w0, unsigned w1, int bufoff) {
    f.x[0] = *(LAS const bf16x8*)(uintptr_t)(xa);
#pragma unroll
    for (int t = 0; t < 7; ++t)
        f.w[t] = *(LAS const bf16x8*)(uintptr_t)(((t & 1) ? w1 : w0) + (unsigned)(t * 2048 + bufoff));
#pragma unroll
    for (int m = 1; m < 4; ++m)
        f.x[m] = *(LAS const bf16x8*)(uintptr_t)(xa + (unsigned)(m * 2048));
}
DI void mma(f32x4 (&acc)[4][7], const Frag& f) {
#pragma unroll
    for (int m = 0; m < 4; ++m)
#pragma unroll
        for (int t = 0; t < 7; ++t)
            acc[m][t] = __builtin_amdgcn_mfma_f32_16x16x32_bf16(f.w[t], f.x[m], acc[m][t], 0, 0, 0);
}
DI void interleave() {
#pragma unroll
    for (int i = 0; i < 11; ++i) {
        __builtin_amdgcn_sched_group_barrier(0x008, 2, 0);
        __builtin_amdgcn_sched_group_barrier(0x100, 1, 0);
    }
    __builtin_amdgcn_sched_group_barrier(0x008, 6, 0);
}

__global__ __launch_bounds__(512, 2)
void w4a16_kernel(const unsigned short* __restrict__ x, const int* __restrict__ qw, const unsigned short* __restrict__ sc,
                  const unsigned short* __restrict__ zr, unsigned short* __restrict__ out, void* ws) {
    __shared__ __attribute__((aligned(1024))) char smem[LDS_TOTAL];
    const unsigned lbase = (unsigned)(uintptr_t)(LAS char*)smem;
    const int lane = threadIdx.x & 63;
    const int wid = __builtin_amdgcn_readfirstlane(threadIdx.x >> 6);
    const int bid = blockIdx.x, xcd = bid & 7, idx = bid >> 3;
    const int tm = idx & 7, tn = xcd * 4 + (idx >> 3);
    const int m0 = tm * BM, n0 = tn * BN;
    STAMP_DECL;
    STAMP(0);

    if (wid >= 4) {
        const int pw = wid - 4;
        PCtx c;
        c.srdQ = __builtin_amdgcn_make_buffer_rsrc((void*)qw, (short)0, K_ * N_ * 4, 0x00020000);
        c.srdX = __builtin_amdgcn_make_buffer_rsrc((void*)x, (short)0, M_ * K_ * 2, 0x00020000);
        const int kb = 2 * pw + (lane >> 5), nb = lane & 31;
        c.act = nb < 28;
        c.vq = c.act ? (unsigned)(kb * 8 * QROW + (n0 + 4 * nb) * 4) : 0x80000000u;
        const int rin = pw * 8 + (lane >> 3);
        c.vx = (unsigned)((m0 + rin) * XROW + (((lane & 7) ^ ((rin >> 1) & 7)) * 16));
        c.m0a = lbase + LDS_A + pw * 1024;
        u32x4 q0[8], q1[8];
        const unsigned pq = lane < 56 ? (unsigned)((lane / 14) * QROW + xcd * 1792 + (lane % 14) * 128) : 0x80000000u;
        const unsigned pxg = (unsigned)((bid * 8 + (lane & 7)) * XROW + (lane >> 3) * 128);
        const unsigned sink = lbase + LDS_PF;
#pragma unroll
        for (int i = 0; i < 8; ++i) ld_q(q0[i], c.vq, c.srdQ, (unsigned)i * QROW);
#pragma unroll
        for (int j = 0; j < 8; ++j) dma16(c.m0a + (unsigned)(j * 4096), c.vx, c.srdX, (unsigned)(j * 32 * XROW));
        if (PFD) {
            if (pw < 2) dma4(sink, pq, c.srdQ, (unsigned)((pw * 128 + 64 + idx * 4) * QROW));
            else        dma4(sink, pxg, c.srdX, (unsigned)((pw - 2) * 1024));
        }
#pragma unroll
        for (int i = 0; i < 8; ++i) ld_q(q1[i], c.vq, c.srdQ, (unsigned)(BK + i) * QROW);
#pragma unroll
        for (int j = 0; j < 8; ++j) dma16(c.m0a + (unsigned)(A_STAGE + j * 4096), c.vx, c.srdX, (unsigned)(BK * 2 + j * 32 * XROW));
        const int rb = nb < 24 ? (32 * (nb >> 3) + 16 * (nb & 1) + 4 * ((nb >> 1) & 3)) : (96 + 4 * (nb - 24));
        const int f = ((rb >> 1) & 7) ^ ((rb >> 4) & 1);
        c.wa0 = lbase + (unsigned)(LDS_B + rb * 128 + ((kb ^ f) * 16));
        c.wa1 = c.wa0 ^ 16u;
        unsigned sza = lbase + (unsigned)(LDS_SZ + (c.act ? nb : 0) * 16);
        BAR();
        STAMP(1);
        float S[4], C[4];
#define LOAD_SCALES() do { const u32x4 sz = *(LAS const u32x4*)(uintptr_t)sza; sza += BN * 4; \
            _Pragma("unroll") for (int j = 0; j < 4; ++j) { \
                const float s = __uint_as_float(sz[j] << 16), z = __uint_as_float(sz[j] & 0xffff0000u); \
                S[j] = fmul(s, 16777216.0f); C[j] = fmul(-z, s); } } while (0)
#define PF_TURN(b) do { if (PFD && (b) + 5 < NIT && ((b) & 7) == (idx & 7)) dma4(sink, pq, c.srdQ, (unsigned)((((b) + 5) * 64 + (idx >> 3) * 16 + pw * 4) * QROW)); } while (0)
        LOAD_SCALES();
        for (int b = 0; b < NIT - 2; b += 2) {
            pstep<true, 20>(q0, S, C, c, 0, b + 2);
            PF_TURN(b);
            WAIT_L0();
            BAR();
            LSTAMP(2 + b);
            pstep<true, 20>(q1, S, C, c, B_STAGE, b + 3);
            PF_TURN(b + 1);
            LOAD_SCALES();
            WAIT_L0();
            BAR();
            LSTAMP(3 + b);
        }
        pstep<false, 16>(q0, S, C, c, 0, 0);
        WAIT_L0();
        BAR();
        LSTAMP(16);
        pstep<false, 0>(q1, S, C, c, B_STAGE, 0);
        WAIT_L0();
        BAR();
        STAMP(17);
        BAR();
    } else {
        const int w = wid;
        {
            const int t = threadIdx.x;
            if (t < NG * (BN / 4)) {
                const int g = t / (BN / 4), c4 = t % (BN / 4);
                const u32x2 s = *(const u32x2*)(sc + g * N_ + n0 + c4 * 4);
                const u32x2 z = *(const u32x2*)(zr + g * N_ + n0 + c4 * 4);
                u32x4 o;
                o[0] = (s[0] & 0xffffu) | (z[0] << 16);
                o[1] = (s[0] >> 16) | (z[0] & 0xffff0000u);
                o[2] = (s[1] & 0xffffu) | (z[1] << 16);
                o[3] = (s[1] >> 16) | (z[1] & 0xffff0000u);
                *(LAS u32x4*)(uintptr_t)(lbase + LDS_SZ + t * 16) = o;
            }
            asm volatile("" :: "s"(out));
            __builtin_amdgcn_s_waitcnt(0xC07F);
            BAR();
            STAMP(1);
        }
        const int fr = lane & 15, fq = lane >> 4;
        const unsigned sw = (unsigned)((fq ^ (fr >> 1)) * 16);
        unsigned xa0 = lbase + (unsigned)(LDS_A + (w * 64 + fr) * 128) + sw;
        unsigned w00 = lbase + (unsigned)(LDS_B + fr * 128) + sw;
        unsigned w01 = w00 ^ 16u, w10 = w00 ^ 64u, w11 = w00 ^ 80u;
        asm volatile("" : "+v"(w00), "+v"(w01), "+v"(w10), "+v"(w11));
        f32x4 acc[4][7];
#pragma unroll
        for (int m = 0; m < 4; ++m)
#pragma unroll
            for (int t = 0; t < 7; ++t) { acc[m][t] = (f32x4){0.f, 0.f, 0.f, 0.f}; asm volatile("" : "+v"(acc[m][t])); }
        Frag f0, f1;
        BAR();
        STAMP(2);
        read_frags(f0, xa0, w00, w01, 0);
        for (int it = 0; it < NIT; it += 2) {
            unsigned xe = xa0 + (unsigned)((it & 2) * A_STAGE);
            unsigned xn = xa0 + (unsigned)(((it + 2) & 2) * A_STAGE);
            asm volatile("" : "+v"(xe), "+v"(xn));
            SB0();
            read_frags(f1, xe ^ 64u, w10, w11, 0);
            mma(acc, f0);
            interleave();
            SB0();
            WAIT_L0();
            BAR();
            LSTAMP(3 + it);
            SB0();
            read_frags(f0, xe + A_STAGE, w00, w01, B_STAGE);
            mma(acc, f1);
            interleave();
            SB0();
            read_frags(f1, (xe ^ 64u) + A_STAGE, w10, w11, B_STAGE);
            mma(acc, f0);
            interleave();
            SB0();
            WAIT_L0();
            BAR();
            LSTAMP(4 + it);
            SB0();
            read_frags(f0, xn, w00, w01, 0);
            mma(acc, f1);
            interleave();
            SB0();
        }
        STAMP(19);
        const unsigned eb = lbase + (unsigned)(A_STAGE + w * (64 * OROW));
        const unsigned ew = eb + (unsigned)(fr * OROW + fq * 16);
#pragma unroll
        for (int m = 0; m < 4; ++m) {
#pragma unroll
            for (int pp = 0; pp < 3; ++pp) {
                u32x4 v;
                v[0] = cvt_pk_bf16(acc[m][2 * pp][0], acc[m][2 * pp][1]);
                v[1] = cvt_pk_bf16(acc[m][2 * pp][2], acc[m][2 * pp][3]);
                v[2] = cvt_pk_bf16(acc[m][2 * pp + 1][0], acc[m][2 * pp + 1][1]);
                v[3] = cvt_pk_bf16(acc[m][2 * pp + 1][2], acc[m][2 * pp + 1][3]);
                *(LAS u32x4*)(uintptr_t)(ew + (unsigned)(m * 16 * OROW + pp * 64)) = v;
            }
            u32x2 v;
            v[0] = cvt_pk_bf16(acc[m][6][0], acc[m][6][1]);
            v[1] = cvt_pk_bf16(acc[m][6][2], acc[m][6][3]);
            *(LAS u32x2*)(uintptr_t)(ew - (unsigned)(fq * 8) + (unsigned)(m * 16 * OROW + 192)) = v;
        }
        char* obase = (char*)(out + (size_t)(m0 + w * 64) * N_ + n0);
#pragma unroll
        for (int i = 0; i < 14; ++i) {
            const int cix = i * 64 + lane;
            const int row = (cix * 4682) >> 16, col = cix - row * 14;
            const u32x4 v = *(LAS const u32x4*)(uintptr_t)(eb + (unsigned)(row * OROW + col * 16));
            *(u32x4*)(obase + (unsigned)(row * (N_ * 2) + col * 16)) = v;
        }
        STAMP(20);
        STAMP(21);
    }
    STAMP_OUT();
}
}

extern "C" void kernel_launch(void* const* d_in, const int* in_sizes, int n_in,
                              void* d_out, int out_size, void* d_ws, size_t ws_size,
                              hipStream_t stream) {
    w4a16_kernel<<<256, 512, 0, stream>>>((const unsigned short*)d_in[0], (const int*)d_in[1],
        (const unsigned short*)d_in[2], (const unsigned short*)d_in[3], (unsigned short*)d_out, d_ws);
}
```

```cpp
#define DIAG 0
#include <hip/hip_runtime.h>
#include <hip/hip_bf16.h>

typedef short bf16x8 __attribute__((ext_vector_type(8)));
typedef float f32x4 __attribute__((ext_vector_type(4)));
typedef unsigned u32x4 __attribute__((ext_vector_type(4)));
typedef unsigned u32x2 __attribute__((ext_vector_type(2)));
#define LAS __attribute__((address_space(3)))
#define DI __device__ __forceinline__

#define PFD 1
#define TLBW 0
#define EPI 1

namespace {
constexpr int M_ = 2048, N_ = 3584, K_ = 1024, G_ = 128, NG = K_ / G_;
constexpr int BM = 256, BN = 112, BK = 64, NIT = K_ / BK;
constexpr int A_STAGE = BM * BK * 2;
constexpr int B_STAGE = BN * BK * 2;
constexpr int LDS_A = 0, LDS_B = 4 * A_STAGE, LDS_SZ = LDS_B + 2 * B_STAGE;
constexpr int LDS_PF = LDS_SZ + NG * BN * 4;
constexpr int LDS_TOTAL = LDS_PF + 256;
constexpr int QROW = N_ * 4;
constexpr int XROW = K_ * 2;
constexpr int OROW = 240;

#define STAMP(i)
#define STAMP_DECL
#define STAMP_OUT()
#define LSTAMP(i) STAMP(i)
#define WAIT_V(n) asm volatile("s_waitcnt vmcnt(%0)" ::"n"(n) : "memory")
#define WAIT_L0() asm volatile("s_waitcnt lgkmcnt(0)" ::: "memory")
#define BAR() do { asm volatile("" ::: "memory"); __builtin_amdgcn_s_barrier(); asm volatile("" ::: "memory"); } while (0)
#define SB0() __builtin_amdgcn_sched_barrier(0)

DI unsigned cvt_pk_bf16(float lo, float hi) { unsigned r; asm("v_cvt_pk_bf16_f32 %0, %1, %2" : "=v"(r) : "v"(lo), "v"(hi)); return r; }

DI float fmul(float a, float b) { float r; asm volatile("v_mul_f32 %0, %1, %2" : "=v"(r) : "v"(a), "v"(b)); return r; }
DI void ld_q(u32x4& q, unsigned vq, __amdgpu_buffer_rsrc_t srd, unsigned so) {
    asm volatile("buffer_load_dwordx4 %0, %1, %2, %3 offen" : "=v"(q) : "v"(vq), "s"(srd), "s"(so) : "memory");
}
DI void dma16(unsigned m0v, unsigned v, __amdgpu_buffer_rsrc_t srd, unsigned so) {
    asm volatile("s_mov_b32 m0, %0\n\ts_nop 0\n\tbuffer_load_dwordx4 %1, %2, %3 offen lds" :: "s"(m0v), "v"(v), "s"(srd), "s"(so) : "memory");
}
DI void dma4(unsigned m0v, unsigned v, __amdgpu_buffer_rsrc_t srd, unsigned so) {
    asm volatile("s_mov_b32 m0, %0\n\ts_nop 0\n\tbuffer_load_dword %1, %2, %3 offen lds" :: "s"(m0v), "v"(v), "s"(srd), "s"(so) : "memory");
}
template <int W> DI void wait_B(u32x4 (&q)[8]) {
    asm volatile("s_waitcnt vmcnt(%8)"
                 : "+v"(q[0]), "+v"(q[1]), "+v"(q[2]), "+v"(q[3]), "+v"(q[4]), "+v"(q[5]), "+v"(q[6]), "+v"(q[7]) : "n"(W) : "memory");
}
DI void dequant_pair(u32x4 (&o)[4], int kk, const u32x4& qa, const u32x4& qb, const float (&S)[4], const float (&C)[4]) {
    float t0, t1, t2, t3, t4, t5, t6, t7;
    asm volatile(
        "v_fma_mix_f32 %4, %12, %20, %24 op_sel_hi:[1,0,0]\n\t"
        "v_fma_mix_f32 %5, %16, %20, %24 op_sel_hi:[1,0,0]\n\t"
        "v_fma_mix_f32 %6, %13, %21, %25 op_sel_hi:[1,0,0]\n\t"
        "v_fma_mix_f32 %7, %17, %21, %25 op_sel_hi:[1,0,0]\n\t"
        "v_fma_mix_f32 %8, %14, %22, %26 op_sel_hi:[1,0,0]\n\t"
        "v_fma_mix_f32 %9, %18, %22, %26 op_sel_hi:[1,0,0]\n\t"
        "v_fma_mix_f32 %10, %15, %23, %27 op_sel_hi:[1,0,0]\n\t"
        "v_fma_mix_f32 %11, %19, %23, %27 op_sel_hi:[1,0,0]\n\t"
        "v_cvt_pk_bf16_f32 %0, %4, %5\n\t"
        "v_cvt_pk_bf16_f32 %1, %6, %7\n\t"
        "v_cvt_pk_bf16_f32 %2, %8, %9\n\t"
        "v_cvt_pk_bf16_f32 %3, %10, %11"
        : "=&v"(o[0][kk]), "=&v"(o[1][kk]), "=&v"(o[2][kk]), "=&v"(o[3][kk]),
          "=&v"(t0), "=&v"(t1), "=&v"(t2), "=&v"(t3), "=&v"(t4), "=&v"(t5), "=&v"(t6), "=&v"(t7)
        : "v"(qa[0]), "v"(qa[1]), "v"(qa[2]), "v"(qa[3]), "v"(qb[0]), "v"(qb[1]), "v"(qb[2]), "v"(qb[3]),
          "v"(S[0]), "v"(S[1]), "v"(S[2]), "v"(S[3]), "v"(C[0]), "v"(C[1]), "v"(C[2]), "v"(C[3]));
}
struct PCtx { unsigned vq, vx, m0a, wa0, wa1; __amdgpu_buffer_rsrc_t srdQ, srdX; bool act; };
template <bool ISSUE, int WV>
DI void pstep(u32x4 (&q)[8], const float (&S)[4], const float (&C)[4], const PCtx& c, int bufoff, int ns) {
    u32x4 o[4];
    const unsigned sq = (unsigned)ns * (BK * QROW), sx = (unsigned)ns * (BK * 2), mb = c.m0a + (unsigned)(ns & 3) * A_STAGE;
    if constexpr (ISSUE) {
#pragma unroll
        for (int j = 0; j < 4; ++j) dma16(mb + (unsigned)(j * 4096), c.vx, c.srdX, sx + (unsigned)(j * 32 * XROW));
    }
    wait_B<WV>(q);
#pragma unroll
    for (int kk = 0; kk < 4; ++kk) {
        dequant_pair(o, kk, q[2 * kk], q[2 * kk + 1], S, C);
        if (kk == 3 && c.act) {
#pragma unroll
            for (int j = 0; j < 4; ++j) *(LAS u32x4*)(uintptr_t)(((j >> 1) ? c.wa1 : c.wa0) + (unsigned)(j * 128 + bufoff)) = o[j];
        }
        if constexpr (ISSUE) {
            ld_q(q[2 * kk], c.vq, c.srdQ, sq + (unsigned)(2 * kk) * QROW);
            ld_q(q[2 * kk + 1], c.vq, c.srdQ, sq + (unsigned)(2 * kk + 1) * QROW);
        }
    }
    if constexpr (ISSUE) {
#pragma unroll
        for (int j = 4; j < 8; ++j) dma16(mb + (unsigned)(j * 4096), c.vx, c.srdX, sx + (unsigned)(j * 32 * XROW));
    }
}

struct Frag { bf16x8 x[4]; bf16x8 w[7]; };
DI void read_frags(Frag& f, unsigned xa, unsigned w0, unsigned w1, int bufoff) {
    f.x[0] = *(LAS const bf16x8*)(uintptr_t)(xa);
#pragma unroll
    for (int t = 0; t < 7; ++t)
        f.w[t] = *(LAS const bf16x8*)(uintptr_t)(((t & 1) ? w1 : w0) + (unsigned)(t * 2048 + bufoff));
#pragma unroll
    for (int m = 1; m < 4; ++m)
        f.x[m] = *(LAS const bf16x8*)(uintptr_t)(xa + (unsigned)(m * 2048));
}
DI void mma(f32x4 (&acc)[4][7], const Frag& f) {
#pragma unroll
    for (int m = 0; m < 4; ++m)
#pragma unroll
        for (int t = 0; t < 7; ++t)
            acc[m][t] = __builtin_amdgcn_mfma_f32_16x16x32_bf16(f.w[t], f.x[m], acc[m][t], 0, 0, 0);
}
DI void interleave() {
#pragma unroll
    for (int i = 0; i < 11; ++i) {
        __builtin_amdgcn_sched_group_barrier(0x008, 2, 0);
        __builtin_amdgcn_sched_group_barrier(0x100, 1, 0);
    }
    __builtin_amdgcn_sched_group_barrier(0x008, 6, 0);
}

__global__ __launch_bounds__(512, 2)
void w4a16_kernel(const unsigned short* __restrict__ x, const int* __restrict__ qw, const unsigned short* __restrict__ sc,
                  const unsigned short* __restrict__ zr, unsigned short* __restrict__ out, void* ws) {
    __shared__ __attribute__((aligned(1024))) char smem[LDS_TOTAL];
    const unsigned lbase = (unsigned)(uintptr_t)(LAS char*)smem;
    const int lane = threadIdx.x & 63;
    const int wid = __builtin_amdgcn_readfirstlane(threadIdx.x >> 6);
    const int bid = blockIdx.x, xcd = bid & 7, idx = bid >> 3;
    const int tm = idx & 7, tn = xcd * 4 + (idx >> 3);
    const int m0 = tm * BM, n0 = tn * BN;
    STAMP_DECL;
    STAMP(0);
    unsigned long long dg0, dg2, dg19, dg21, dc2, dc19;
#define RT(v) asm volatile("s_memrealtime %0\n\ts_waitcnt lgkmcnt(0)" : "=s"(v) :: "memory")
#define CT(v) asm volatile("s_memtime %0\n\ts_waitcnt lgkmcnt(0)" : "=s"(v) :: "memory")
    RT(dg0);

    if (wid >= 4) {
        const int pw = wid - 4;
        PCtx c;
        c.srdQ = __builtin_amdgcn_make_buffer_rsrc((void*)qw, (short)0, K_ * N_ * 4, 0x00020000);
        c.srdX = __builtin_amdgcn_make_buffer_rsrc((void*)x, (short)0, M_ * K_ * 2, 0x00020000);
        const int kb = 2 * pw + (lane >> 5), nb = lane & 31;
        c.act = nb < 28;
        c.vq = c.act ? (unsigned)(kb * 8 * QROW + (n0 + 4 * nb) * 4) : 0x80000000u;
        const int rin = pw * 8 + (lane >> 3);
        c.vx = (unsigned)((m0 + rin) * XROW + (((lane & 7) ^ ((rin >> 1) & 7)) * 16));
        c.m0a = lbase + LDS_A + pw * 1024;
        u32x4 q0[8], q1[8];
        const unsigned pq = lane < 56 ? (unsigned)((lane / 14) * QROW + xcd * 1792 + (lane % 14) * 128) : 0x80000000u;
        const unsigned pxg = (unsigned)((bid * 8 + (lane & 7)) * XROW + (lane >> 3) * 128);
        const unsigned sink = lbase + LDS_PF;
#pragma unroll
        for (int i = 0; i < 8; ++i) ld_q(q0[i], c.vq, c.srdQ, (unsigned)i * QROW);
#pragma unroll
        for (int j = 0; j < 8; ++j) dma16(c.m0a + (unsigned)(j * 4096), c.vx, c.srdX, (unsigned)(j * 32 * XROW));
        if (PFD) {
            if (pw < 2) dma4(sink, pq, c.srdQ, (unsigned)((pw * 128 + 64 + idx * 4) * QROW));
            else        dma4(sink, pxg, c.srdX, (unsigned)((pw - 2) * 1024));
        }
#pragma unroll
        for (int i = 0; i < 8; ++i) ld_q(q1[i], c.vq, c.srdQ, (unsigned)(BK + i) * QROW);
#pragma unroll
        for (int j = 0; j < 8; ++j) dma16(c.m0a + (unsigned)(A_STAGE + j * 4096), c.vx, c.srdX, (unsigned)(BK * 2 + j * 32 * XROW));
        const int rb = nb < 24 ? (32 * (nb >> 3) + 16 * (nb & 1) + 4 * ((nb >> 1) & 3)) : (96 + 4 * (nb - 24));
        const int f = ((rb >> 1) & 7) ^ ((rb >> 4) & 1);
        c.wa0 = lbase + (unsigned)(LDS_B + rb * 128 + ((kb ^ f) * 16));
        c.wa1 = c.wa0 ^ 16u;
        unsigned sza = lbase + (unsigned)(LDS_SZ + (c.act ? nb : 0) * 16);
        BAR();
        STAMP(1);
        float S[4], C[4];
#define LOAD_SCALES() do { const u32x4 sz = *(LAS const u32x4*)(uintptr_t)sza; sza += BN * 4; \
            _Pragma("unroll") for (int j = 0; j < 4; ++j) { \
                const float s = __uint_as_float(sz[j] << 16), z = __uint_as_float(sz[j] & 0xffff0000u); \
                S[j] = fmul(s, 16777216.0f); C[j] = fmul(-z, s); } } while (0)
#define PF_TURN(b) do { if (PFD && (b) + 5 < NIT && ((b) & 7) == (idx & 7)) dma4(sink, pq, c.srdQ, (unsigned)((((b) + 5) * 64 + (idx >> 3) * 16 + pw * 4) * QROW)); } while (0)
        LOAD_SCALES();
        for (int b = 0; b < NIT - 2; b += 2) {
            pstep<true, 20>(q0, S, C, c, 0, b + 2);
            PF_TURN(b);
            WAIT_L0();
            BAR();
            LSTAMP(2 + b);
            pstep<true, 20>(q1, S, C, c, B_STAGE, b + 3);
            PF_TURN(b + 1);
            LOAD_SCALES();
            WAIT_L0();
            BAR();
            LSTAMP(3 + b);
        }
        pstep<false, 16>(q0, S, C, c, 0, 0);
        WAIT_L0();
        BAR();
        LSTAMP(16);
        pstep<false, 0>(q1, S, C, c, B_STAGE, 0);
        WAIT_L0();
        BAR();
        STAMP(17);
        BAR();
    } else {
        const int w = wid;
        {
            const int t = threadIdx.x;
            if (t < NG * (BN / 4)) {
                const int g = t / (BN / 4), c4 = t % (BN / 4);
                const u32x2 s = *(const u32x2*)(sc + g * N_ + n0 + c4 * 4);
                const u32x2 z = *(const u32x2*)(zr + g * N_ + n0 + c4 * 4);
                u32x4 o;
                o[0] = (s[0] & 0xffffu) | (z[0] << 16);
                o[1] = (s[0] >> 16) | (z[0] & 0xffff0000u);
                o[2] = (s[1] & 0xffffu) | (z[1] << 16);
                o[3] = (s[1] >> 16) | (z[1] & 0xffff0000u);
                *(LAS u32x4*)(uintptr_t)(lbase + LDS_SZ + t * 16) = o;
            }
            asm volatile("" :: "s"(out));
            __builtin_amdgcn_s_waitcnt(0xC07F);
            BAR();
            STAMP(1);
        }
        const int fr = lane & 15, fq = lane >> 4;
        const unsigned sw = (unsigned)((fq ^ (fr >> 1)) * 16);
        unsigned xa0 = lbase + (unsigned)(LDS_A + (w * 64 + fr) * 128) + sw;
        unsigned w00 = lbase + (unsigned)(LDS_B + fr * 128) + sw;
        unsigned w01 = w00 ^ 16u, w10 = w00 ^ 64u, w11 = w00 ^ 80u;
        asm volatile("" : "+v"(w00), "+v"(w01), "+v"(w10), "+v"(w11));
        f32x4 acc[4][7];
#pragma unroll
        for (int m = 0; m < 4; ++m)
#pragma unroll
            for (int t = 0; t < 7; ++t) { acc[m][t] = (f32x4){0.f, 0.f, 0.f, 0.f}; asm volatile("" : "+v"(acc[m][t])); }
        Frag f0, f1;
        BAR();
        STAMP(2);
        RT(dg2); CT(dc2);
        read_frags(f0, xa0, w00, w01, 0);
        for (int it = 0; it < NIT; it += 2) {
            unsigned xe = xa0 + (unsigned)((it & 2) * A_STAGE);
            unsigned xn = xa0 + (unsigned)(((it + 2) & 2) * A_STAGE);
            asm volatile("" : "+v"(xe), "+v"(xn));
            SB0();
            read_frags(f1, xe ^ 64u, w10, w11, 0);
            mma(acc, f0);
            interleave();
            SB0();
            WAIT_L0();
            BAR();
            LSTAMP(3 + it);
            SB0();
            read_frags(f0, xe + A_STAGE, w00, w01, B_STAGE);
            mma(acc, f1);
            interleave();
            SB0();
            read_frags(f1, (xe ^ 64u) + A_STAGE, w10, w11, B_STAGE);
            mma(acc, f0);
            interleave();
            SB0();
            WAIT_L0();
            BAR();
            LSTAMP(4 + it);
            SB0();
            read_frags(f0, xn, w00, w01, 0);
            mma(acc, f1);
            interleave();
            SB0();
        }
        STAMP(19);
        SB0(); RT(dg19); CT(dc19); SB0();
        const unsigned eb = lbase + (unsigned)(A_STAGE + w * (64 * OROW));
        const unsigned ew = eb + (unsigned)(fr * OROW + fq * 16);
#pragma unroll
        for (int m = 0; m < 4; ++m) {
#pragma unroll
            for (int pp = 0; pp < 3; ++pp) {
                u32x4 v;
                v[0] = cvt_pk_bf16(acc[m][2 * pp][0], acc[m][2 * pp][1]);
                v[1] = cvt_pk_bf16(acc[m][2 * pp][2], acc[m][2 * pp][3]);
                v[2] = cvt_pk_bf16(acc[m][2 * pp + 1][0], acc[m][2 * pp + 1][1]);
                v[3] = cvt_pk_bf16(acc[m][2 * pp + 1][2], acc[m][2 * pp + 1][3]);
                *(LAS u32x4*)(uintptr_t)(ew + (unsigned)(m * 16 * OROW + pp * 64)) = v;
            }
            u32x2 v;
            v[0] = cvt_pk_bf16(acc[m][6][0], acc[m][6][1]);
            v[1] = cvt_pk_bf16(acc[m][6][2], acc[m][6][3]);
            *(LAS u32x2*)(uintptr_t)(ew - (unsigned)(fq * 8) + (unsigned)(m * 16 * OROW + 192)) = v;
        }
        char* obase = (char*)(out + (size_t)(m0 + w * 64) * N_ + n0);
#pragma unroll
        for (int i = 0; i < 14; ++i) {
            const int cix = i * 64 + lane;
            const int row = (cix * 4682) >> 16, col = cix - row * 14;
            const u32x4 v = *(LAS const u32x4*)(uintptr_t)(eb + (unsigned)(row * OROW + col * 16));
            *(u32x4*)(obase + (unsigned)(row * (N_ * 2) + col * 16)) = v;
        }
        STAMP(20);
        STAMP(21);
        WAIT_V(0);
        RT(dg21);
        { const unsigned long long q = DIAG == 1 ? dg2 - dg0 : DIAG == 2 ? dg19 - dg2 : DIAG == 3 ? dg21 - dg19 : DIAG == 4 ? (dc19 - dc2) / 24 : DIAG == 5 ? dg21 - dg0 : 0;
          unsigned long long now; do { __builtin_amdgcn_s_sleep(4); RT(now); } while (now - dg21 < 8 * q); }
    }
    STAMP_OUT();
}
}

extern "C" void kernel_launch(void* const* d_in, const int* in_sizes, int n_in,
                              void* d_out, int out_size, void* d_ws, size_t ws_size,
                              hipStream_t stream) {
    w4a16_kernel<<<256, 512, 0, stream>>>((const unsigned short*)d_in[0], (const int*)d_in[1],
        (const unsigned short*)d_in[2], (const unsigned short*)d_in[3], (unsigned short*)d_out, d_ws);
}
```

```cpp
#include <hip/hip_runtime.h>
#include <hip/hip_bf16.h>

typedef short bf16x8 __attribute__((ext_vector_type(8)));
typedef float f32x4 __attribute__((ext_vector_type(4)));
typedef unsigned u32x4 __attribute__((ext_vector_type(4)));
typedef unsigned u32x2 __attribute__((ext_vector_type(2)));
#define LAS __attribute__((address_space(3)))
#define DI __device__ __forceinline__

#define PFD 1
#define TLBW 0
#define STAUX 2
#define EPI 1

namespace {
constexpr int M_ = 2048, N_ = 3584, K_ = 1024, G_ = 128, NG = K_ / G_;
constexpr int BM = 256, BN = 112, BK = 64, NIT = K_ / BK;
constexpr int A_STAGE = BM * BK * 2;
constexpr int B_STAGE = BN * BK * 2;
constexpr int LDS_A = 0, LDS_B = 4 * A_STAGE, LDS_SZ = LDS_B + 2 * B_STAGE;
constexpr int LDS_PF = LDS_SZ + NG * BN * 4;
constexpr int LDS_TOTAL = LDS_PF + 256;
constexpr int QROW = N_ * 4;
constexpr int XROW = K_ * 2;
constexpr int OROW = 240;

#define STAMP(i)
#define STAMP_DECL
#define STAMP_OUT()
#define LSTAMP(i) STAMP(i)
#define WAIT_V(n) asm volatile("s_waitcnt vmcnt(%0)" ::"n"(n) : "memory")
#define WAIT_L0() asm volatile("s_waitcnt lgkmcnt(0)" ::: "memory")
#define BAR() do { asm volatile("" ::: "memory"); __builtin_amdgcn_s_barrier(); asm volatile("" ::: "memory"); } while (0)
#define SB0() __builtin_amdgcn_sched_barrier(0)

DI unsigned cvt_pk_bf16(float lo, float hi) { unsigned r; asm("v_cvt_pk_bf16_f32 %0, %1, %2" : "=v"(r) : "v"(lo), "v"(hi)); return r; }

DI float fmul(float a, float b) { float r; asm volatile("v_mul_f32 %0, %1, %2" : "=v"(r) : "v"(a), "v"(b)); return r; }
DI void ld_q(u32x4& q, unsigned vq, __amdgpu_buffer_rsrc_t srd, unsigned so) {
    asm volatile("buffer_load_dwordx4 %0, %1, %2, %3 offen" : "=v"(q) : "v"(vq), "s"(srd), "s"(so) : "memory");
}
DI void dma16(unsigned m0v, unsigned v, __amdgpu_buffer_rsrc_t srd, unsigned so) {
    asm volatile("s_mov_b32 m0, %0\n\ts_nop 0\n\tbuffer_load_dwordx4 %1, %2, %3 offen lds" :: "s"(m0v), "v"(v), "s"(srd), "s"(so) : "memory");
}
DI void dma4(unsigned m0v, unsigned v, __amdgpu_buffer_rsrc_t srd, unsigned so) {
    asm volatile("s_mov_b32 m0, %0\n\ts_nop 0\n\tbuffer_load_dword %1, %2, %3 offen lds" :: "s"(m0v), "v"(v), "s"(srd), "s"(so) : "memory");
}
template <int W> DI void wait_B(u32x4 (&q)[8]) {
    asm volatile("s_waitcnt vmcnt(%8)"
                 : "+v"(q[0]), "+v"(q[1]), "+v"(q[2]), "+v"(q[3]), "+v"(q[4]), "+v"(q[5]), "+v"(q[6]), "+v"(q[7]) : "n"(W) : "memory");
}
DI void dequant_pair(u32x4 (&o)[4], int kk, const u32x4& qa, const u32x4& qb, const float (&S)[4], const float (&C)[4]) {
    float t0, t1, t2, t3, t4, t5, t6, t7;
    asm volatile(
        "v_fma_mix_f32 %4, %12, %20, %24 op_sel_hi:[1,0,0]\n\t"
        "v_fma_mix_f32 %5, %16, %20, %24 op_sel_hi:[1,0,0]\n\t"
        "v_fma_mix_f32 %6, %13, %21, %25 op_sel_hi:[1,0,0]\n\t"
        "v_fma_mix_f32 %7, %17, %21, %25 op_sel_hi:[1,0,0]\n\t"
        "v_fma_mix_f32 %8, %14, %22, %26 op_sel_hi:[1,0,0]\n\t"
        "v_fma_mix_f32 %9, %18, %22, %26 op_sel_hi:[1,0,0]\n\t"
        "v_fma_mix_f32 %10, %15, %23, %27 op_sel_hi:[1,0,0]\n\t"
        "v_fma_mix_f32 %11, %19, %23, %27 op_sel_hi:[1,0,0]\n\t"
        "v_cvt_pk_bf16_f32 %0, %4, %5\n\t"
        "v_cvt_pk_bf16_f32 %1, %6, %7\n\t"
        "v_cvt_pk_bf16_f32 %2, %8, %9\n\t"
        "v_cvt_pk_bf16_f32 %3, %10, %11"
        : "=&v"(o[0][kk]), "=&v"(o[1][kk]), "=&v"(o[2][kk]), "=&v"(o[3][kk]),
          "=&v"(t0), "=&v"(t1), "=&v"(t2), "=&v"(t3), "=&v"(t4), "=&v"(t5), "=&v"(t6), "=&v"(t7)
        : "v"(qa[0]), "v"(qa[1]), "v"(qa[2]), "v"(qa[3]), "v"(qb[0]), "v"(qb[1]), "v"(qb[2]), "v"(qb[3]),
          "v"(S[0]), "v"(S[1]), "v"(S[2]), "v"(S[3]), "v"(C[0]), "v"(C[1]), "v"(C[2]), "v"(C[3]));
}
struct PCtx { unsigned vq, vx, m0a, wa0, wa1; __amdgpu_buffer_rsrc_t srdQ, srdX; bool act; };
template <bool ISSUE, int WV>
DI void pstep(u32x4 (&q)[8], const float (&S)[4], const float (&C)[4], const PCtx& c, int bufoff, int ns) {
    u32x4 o[4];
    const unsigned sq = (unsigned)ns * (BK * QROW), sx = (unsigned)ns * (BK * 2), mb = c.m0a + (unsigned)(ns & 3) * A_STAGE;
    if constexpr (ISSUE) {
#pragma unroll
        for (int j = 0; j < 4; ++j) dma16(mb + (unsigned)(j * 4096), c.vx, c.srdX, sx + (unsigned)(j * 32 * XROW));
    }
    wait_B<WV>(q);
#pragma unroll
    for (int kk = 0; kk < 4; ++kk) {
        dequant_pair(o, kk, q[2 * kk], q[2 * kk + 1], S, C);
        if (kk == 3 && c.act) {
#pragma unroll
            for (int j = 0; j < 4; ++j) *(LAS u32x4*)(uintptr_t)(((j >> 1) ? c.wa1 : c.wa0) + (unsigned)(j * 128 + bufoff)) = o[j];
        }
        if constexpr (ISSUE) {
            ld_q(q[2 * kk], c.vq, c.srdQ, sq + (unsigned)(2 * kk) * QROW);
            ld_q(q[2 * kk + 1], c.vq, c.srdQ, sq + (unsigned)(2 * kk + 1) * QROW);
        }
    }
    if constexpr (ISSUE) {
#pragma unroll
        for (int j = 4; j < 8; ++j) dma16(mb + (unsigned)(j * 4096), c.vx, c.srdX, sx + (unsigned)(j * 32 * XROW));
    }
}

struct Frag { bf16x8 x[4]; bf16x8 w[7]; };
DI void read_frags(Frag& f, unsigned xa, unsigned w0, unsigned w1, int bufoff) {
    f.x[0] = *(LAS const bf16x8*)(uintptr_t)(xa);
#pragma unroll
    for (int t = 0; t < 7; ++t)
        f.w[t] = *(LAS const bf16x8*)(uintptr_t)(((t & 1) ? w1 : w0) + (unsigned)(t * 2048 + bufoff));
#pragma unroll
    for (int m = 1; m < 4; ++m)
        f.x[m] = *(LAS const bf16x8*)(uintptr_t)(xa + (unsigned)(m * 2048));
}
DI void mma(f32x4 (&acc)[4][7], const Frag& f) {
#pragma unroll
    for (int m = 0; m < 4; ++m)
#pragma unroll
        for (int t = 0; t < 7; ++t)
            acc[m][t] = __builtin_amdgcn_mfma_f32_16x16x32_bf16(f.w[t], f.x[m], acc[m][t], 0, 0, 0);
}
DI void interleave() {
#pragma unroll
    for (int i = 0; i < 11; ++i) {
        __builtin_amdgcn_sched_group_barrier(0x008, 2, 0);
        __builtin_amdgcn_sched_group_barrier(0x100, 1, 0);
    }
    __builtin_amdgcn_sched_group_barrier(0x008, 6, 0);
}

__global__ __launch_bounds__(512, 2)
void w4a16_kernel(const unsigned short* __restrict__ x, const int* __restrict__ qw, const unsigned short* __restrict__ sc,
                  const unsigned short* __restrict__ zr, unsigned short* __restrict__ out, void* ws) {
    __shared__ __attribute__((aligned(1024))) char smem[LDS_TOTAL];
    const unsigned lbase = (unsigned)(uintptr_t)(LAS char*)smem;
    const int lane = threadIdx.x & 63;
    const int wid = __builtin_amdgcn_readfirstlane(threadIdx.x >> 6);
    const int bid = blockIdx.x, xcd = bid & 7, idx = bid >> 3;
    const int tm = idx & 7, tn = xcd * 4 + (idx >> 3);
    const int m0 = tm * BM, n0 = tn * BN;
    STAMP_DECL;
    STAMP(0);

    if (wid >= 4) {
        const int pw = wid - 4;
        PCtx c;
        c.srdQ = __builtin_amdgcn_make_buffer_rsrc((void*)qw, (short)0, K_ * N_ * 4, 0x00020000);
        c.srdX = __builtin_amdgcn_make_buffer_rsrc((void*)x, (short)0, M_ * K_ * 2, 0x00020000);
        const int kb = 2 * pw + (lane >> 5), nb = lane & 31;
        c.act = nb < 28;
        c.vq = c.act ? (unsigned)(kb * 8 * QROW + (n0 + 4 * nb) * 4) : 0x80000000u;
        const int rin = pw * 8 + (lane >> 3);
        c.vx = (unsigned)((m0 + rin) * XROW + (((lane & 7) ^ ((rin >> 1) & 7)) * 16));
        c.m0a = lbase + LDS_A + pw * 1024;
        u32x4 q0[8], q1[8];
        const unsigned pq = lane < 56 ? (unsigned)((lane / 14) * QROW + xcd * 1792 + (lane % 14) * 128) : 0x80000000u;
        const unsigned pxg = (unsigned)((bid * 8 + (lane & 7)) * XROW + (lane >> 3) * 128);
        const unsigned sink = lbase + LDS_PF;
#pragma unroll
        for (int i = 0; i < 8; ++i) ld_q(q0[i], c.vq, c.srdQ, (unsigned)i * QROW);
#pragma unroll
        for (int j = 0; j < 8; ++j) dma16(c.m0a + (unsigned)(j * 4096), c.vx, c.srdX, (unsigned)(j * 32 * XROW));
        if (PFD) {
            if (pw < 2) dma4(sink, pq, c.srdQ, (unsigned)((pw * 128 + 64 + idx * 4) * QROW));
            else        dma4(sink, pxg, c.srdX, (unsigned)((pw - 2) * 1024));
        }
#pragma unroll
        for (int i = 0; i < 8; ++i) ld_q(q1[i], c.vq, c.srdQ, (unsigned)(BK + i) * QROW);
#pragma unroll
        for (int j = 0; j < 8; ++j) dma16(c.m0a + (unsigned)(A_STAGE + j * 4096), c.vx, c.srdX, (unsigned)(BK * 2 + j * 32 * XROW));
        const int rb = nb < 24 ? (32 * (nb >> 3) + 16 * (nb & 1) + 4 * ((nb >> 1) & 3)) : (96 + 4 * (nb - 24));
        const int f = ((rb >> 1) & 7) ^ ((rb >> 4) & 1);
        c.wa0 = lbase + (unsigned)(LDS_B + rb * 128 + ((kb ^ f) * 16));
        c.wa1 = c.wa0 ^ 16u;
        unsigned sza = lbase + (unsigned)(LDS_SZ + (c.act ? nb : 0) * 16);
        BAR();
        STAMP(1);
        float S[4], C[4];
#define LOAD_SCALES() do { const u32x4 sz = *(LAS const u32x4*)(uintptr_t)sza; sza += BN * 4; \
            _Pragma("unroll") for (int j = 0; j < 4; ++j) { \
                const float s = __uint_as_float(sz[j] << 16), z = __uint_as_float(sz[j] & 0xffff0000u); \
                S[j] = fmul(s, 16777216.0f); C[j] = fmul(-z, s); } } while (0)
#define PF_TURN(b) do { if (PFD && (b) + 5 < NIT && ((b) & 7) == (idx & 7)) dma4(sink, pq, c.srdQ, (unsigned)((((b) + 5) * 64 + (idx >> 3) * 16 + pw * 4) * QROW)); } while (0)
        LOAD_SCALES();
        for (int b = 0; b < NIT - 2; b += 2) {
            pstep<true, 20>(q0, S, C, c, 0, b + 2);
            PF_TURN(b);
            WAIT_L0();
            BAR();
            LSTAMP(2 + b);
            pstep<true, 20>(q1, S, C, c, B_STAGE, b + 3);
            PF_TURN(b + 1);
            LOAD_SCALES();
            WAIT_L0();
            BAR();
            LSTAMP(3 + b);
        }
        pstep<false, 16>(q0, S, C, c, 0, 0);
        WAIT_L0();
        BAR();
        LSTAMP(16);
        pstep<false, 0>(q1, S, C, c, B_STAGE, 0);
        WAIT_L0();
        BAR();
        STAMP(17);
        BAR();
    } else {
        const int w = wid;
        {
            const int t = threadIdx.x;
            if (t < NG * (BN / 4)) {
                const int g = t / (BN / 4), c4 = t % (BN / 4);
                const u32x2 s = *(const u32x2*)(sc + g * N_ + n0 + c4 * 4);
                const u32x2 z = *(const u32x2*)(zr + g * N_ + n0 + c4 * 4);
                u32x4 o;
                o[0] = (s[0] & 0xffffu) | (z[0] << 16);
                o[1] = (s[0] >> 16) | (z[0] & 0xffff0000u);
                o[2] = (s[1] & 0xffffu) | (z[1] << 16);
                o[3] = (s[1] >> 16) | (z[1] & 0xffff0000u);
                *(LAS u32x4*)(uintptr_t)(lbase + LDS_SZ + t * 16) = o;
            }
            asm volatile("" :: "s"(out));
            __builtin_amdgcn_s_waitcnt(0xC07F);
            BAR();
            STAMP(1);
        }
        const int fr = lane & 15, fq = lane >> 4;
        const unsigned sw = (unsigned)((fq ^ (fr >> 1)) * 16);
        unsigned xa0 = lbase + (unsigned)(LDS_A + (w * 64 + fr) * 128) + sw;
        unsigned w00 = lbase + (unsigned)(LDS_B + fr * 128) + sw;
        unsigned w01 = w00 ^ 16u, w10 = w00 ^ 64u, w11 = w00 ^ 80u;
        asm volatile("" : "+v"(w00), "+v"(w01), "+v"(w10), "+v"(w11));
        f32x4 acc[4][7];
#pragma unroll
        for (int m = 0; m < 4; ++m)
#pragma unroll
            for (int t = 0; t < 7; ++t) { acc[m][t] = (f32x4){0.f, 0.f, 0.f, 0.f}; asm volatile("" : "+v"(acc[m][t])); }
        Frag f0, f1;
        BAR();
        STAMP(2);
        read_frags(f0, xa0, w00, w01, 0);
        for (int it = 0; it < NIT; it += 2) {
            unsigned xe = xa0 + (unsigned)((it & 2) * A_STAGE);
            unsigned xn = xa0 + (unsigned)(((it + 2) & 2) * A_STAGE);
            asm volatile("" : "+v"(xe), "+v"(xn));
            SB0();
            read_frags(f1, xe ^ 64u, w10, w11, 0);
            mma(acc, f0);
            interleave();
            SB0();
            WAIT_L0();
            BAR();
            LSTAMP(3 + it);
            SB0();
            read_frags(f0, xe + A_STAGE, w00, w01, B_STAGE);
            mma(acc, f1);
            interleave();
            SB0();
            read_frags(f1, (xe ^ 64u) + A_STAGE, w10, w11, B_STAGE);
            mma(acc, f0);
            interleave();
            SB0();
            WAIT_L0();
            BAR();
            LSTAMP(4 + it);
            SB0();
            read_frags(f0, xn, w00, w01, 0);
            mma(acc, f1);
            interleave();
            SB0();
        }
        STAMP(19);
        const unsigned eb = lbase + (unsigned)(A_STAGE + w * (64 * OROW));
        const unsigned ew = eb + (unsigned)(fr * OROW + fq * 16);
#pragma unroll
        for (int m = 0; m < 4; ++m) {
#pragma unroll
            for (int pp = 0; pp < 3; ++pp) {
                u32x4 v;
                v[0] = cvt_pk_bf16(acc[m][2 * pp][0], acc[m][2 * pp][1]);
                v[1] = cvt_pk_bf16(acc[m][2 * pp][2], acc[m][2 * pp][3]);
                v[2] = cvt_pk_bf16(acc[m][2 * pp + 1][0], acc[m][2 * pp + 1][1]);
                v[3] = cvt_pk_bf16(acc[m][2 * pp + 1][2], acc[m][2 * pp + 1][3]);
                *(LAS u32x4*)(uintptr_t)(ew + (unsigned)(m * 16 * OROW + pp * 64)) = v;
            }
            u32x2 v;
            v[0] = cvt_pk_bf16(acc[m][6][0], acc[m][6][1]);
            v[1] = cvt_pk_bf16(acc[m][6][2], acc[m][6][3]);
            *(LAS u32x2*)(uintptr_t)(ew - (unsigned)(fq * 8) + (unsigned)(m * 16 * OROW + 192)) = v;
        }
        char* obase = (char*)(out + (size_t)(m0 + w * 64) * N_ + n0);
        const __amdgpu_buffer_rsrc_t srdO = __builtin_amdgcn_make_buffer_rsrc((void*)out, (short)0, M_ * N_ * 2, 0x00020000);
        const int osoff = ((m0 + w * 64) * N_ + n0) * 2;
#pragma unroll
        for (int i = 0; i < 14; ++i) {
            const int cix = i * 64 + lane;
            const int row = (cix * 4682) >> 16, col = cix - row * 14;
            const u32x4 v = *(LAS const u32x4*)(uintptr_t)(eb + (unsigned)(row * OROW + col * 16));
            __builtin_amdgcn_raw_buffer_store_b128(v, srdO, (int)(row * (N_ * 2) + col * 16), osoff, STAUX);
        }
        STAMP(20);
        STAMP(21);
    }
    STAMP_OUT();
}
}

extern "C" void kernel_launch(void* const* d_in, const int* in_sizes, int n_in,
                              void* d_out, int out_size, void* d_ws, size_t ws_size,
                              hipStream_t stream) {
    w4a16_kernel<<<256, 512, 0, stream>>>((const unsigned short*)d_in[0], (const int*)d_in[1],
        (const unsigned short*)d_in[2], (const unsigned short*)d_in[3], (unsigned short*)d_out, d_ws);
}
```

```cpp
#define DIAG 0
#include <hip/hip_runtime.h>
#include <hip/hip_bf16.h>

typedef short bf16x8 __attribute__((ext_vector_type(8)));
typedef float f32x4 __attribute__((ext_vector_type(4)));
typedef unsigned u32x4 __attribute__((ext_vector_type(4)));
typedef unsigned u32x2 __attribute__((ext_vector_type(2)));
#define LAS __attribute__((address_space(3)))
#define DI __device__ __forceinline__

#define PFD 1
#define TLBW 0
#define EPI 1

namespace {
constexpr int M_ = 2048, N_ = 3584, K_ = 1024, G_ = 128, NG = K_ / G_;
constexpr int BM = 256, BN = 112, BK = 64, NIT = K_ / BK;
constexpr int A_STAGE = BM * BK * 2;
constexpr int B_STAGE = BN * BK * 2;
constexpr int LDS_A = 0, LDS_B = 4 * A_STAGE, LDS_SZ = LDS_B + 2 * B_STAGE;
constexpr int LDS_PF = LDS_SZ + NG * BN * 4;
constexpr int LDS_TOTAL = LDS_PF + 256;
constexpr int QROW = N_ * 4;
constexpr int XROW = K_ * 2;
constexpr int OROW = 240;

#define STAMP(i)
#define STAMP_DECL
#define STAMP_OUT()
#define LSTAMP(i) STAMP(i)
#define WAIT_V(n) asm volatile("s_waitcnt vmcnt(%0)" ::"n"(n) : "memory")
#define WAIT_L0() asm volatile("s_waitcnt lgkmcnt(0)" ::: "memory")
#define BAR() do { asm volatile("" ::: "memory"); __builtin_amdgcn_s_barrier(); asm volatile("" ::: "memory"); } while (0)
#define SB0() __builtin_amdgcn_sched_barrier(0)

DI unsigned cvt_pk_bf16(float lo, float hi) { unsigned r; asm("v_cvt_pk_bf16_f32 %0, %1, %2" : "=v"(r) : "v"(lo), "v"(hi)); return r; }

DI float fmul(float a, float b) { float r; asm volatile("v_mul_f32 %0, %1, %2" : "=v"(r) : "v"(a), "v"(b)); return r; }
DI void ld_q(u32x4& q, unsigned vq, __amdgpu_buffer_rsrc_t srd, unsigned so) {
    asm volatile("buffer_load_dwordx4 %0, %1, %2, %3 offen" : "=v"(q) : "v"(vq), "s"(srd), "s"(so) : "memory");
}
DI void dma16(unsigned m0v, unsigned v, __amdgpu_buffer_rsrc_t srd, unsigned so) {
    asm volatile("s_mov_b32 m0, %0\n\ts_nop 0\n\tbuffer_load_dwordx4 %1, %2, %3 offen lds" :: "s"(m0v), "v"(v), "s"(srd), "s"(so) : "memory");
}
DI void dma4(unsigned m0v, unsigned v, __amdgpu_buffer_rsrc_t srd, unsigned so) {
    asm volatile("s_mov_b32 m0, %0\n\ts_nop 0\n\tbuffer_load_dword %1, %2, %3 offen lds" :: "s"(m0v), "v"(v), "s"(srd), "s"(so) : "memory");
}
template <int W> DI void wait_B(u32x4 (&q)[8]) {
    asm volatile("s_waitcnt vmcnt(%8)"
                 : "+v"(q[0]), "+v"(q[1]), "+v"(q[2]), "+v"(q[3]), "+v"(q[4]), "+v"(q[5]), "+v"(q[6]), "+v"(q[7]) : "n"(W) : "memory");
}
DI void dequant_pair(u32x4 (&o)[4], int kk, const u32x4& qa, const u32x4& qb, const float (&S)[4], const float (&C)[4]) {
    float t0, t1, t2, t3, t4, t5, t6, t7;
    asm volatile(
        "v_fma_mix_f32 %4, %12, %20, %24 op_sel_hi:[1,0,0]\n\t"
        "v_fma_mix_f32 %5, %16, %20, %24 op_sel_hi:[1,0,0]\n\t"
        "v_fma_mix_f32 %6, %13, %21, %25 op_sel_hi:[1,0,0]\n\t"
        "v_fma_mix_f32 %7, %17, %21, %25 op_sel_hi:[1,0,0]\n\t"
        "v_fma_mix_f32 %8, %14, %22, %26 op_sel_hi:[1,0,0]\n\t"
        "v_fma_mix_f32 %9, %18, %22, %26 op_sel_hi:[1,0,0]\n\t"
        "v_fma_mix_f32 %10, %15, %23, %27 op_sel_hi:[1,0,0]\n\t"
        "v_fma_mix_f32 %11, %19, %23, %27 op_sel_hi:[1,0,0]\n\t"
        "v_cvt_pk_bf16_f32 %0, %4, %5\n\t"
        "v_cvt_pk_bf16_f32 %1, %6, %7\n\t"
        "v_cvt_pk_bf16_f32 %2, %8, %9\n\t"
        "v_cvt_pk_bf16_f32 %3, %10, %11"
        : "=&v"(o[0][kk]), "=&v"(o[1][kk]), "=&v"(o[2][kk]), "=&v"(o[3][kk]),
          "=&v"(t0), "=&v"(t1), "=&v"(t2), "=&v"(t3), "=&v"(t4), "=&v"(t5), "=&v"(t6), "=&v"(t7)
        : "v"(qa[0]), "v"(qa[1]), "v"(qa[2]), "v"(qa[3]), "v"(qb[0]), "v"(qb[1]), "v"(qb[2]), "v"(qb[3]),
          "v"(S[0]), "v"(S[1]), "v"(S[2]), "v"(S[3]), "v"(C[0]), "v"(C[1]), "v"(C[2]), "v"(C[3]));
}
struct PCtx { unsigned vq, vx, m0a, wa0, wa1; __amdgpu_buffer_rsrc_t srdQ, srdX; bool act; };
template <bool ISSUE, int WV>
DI void pstep(u32x4 (&q)[8], const float (&S)[4], const float (&C)[4], const PCtx& c, int bufoff, int ns) {
    u32x4 o[4];
    const unsigned sq = (unsigned)ns * (BK * QROW), sx = (unsigned)ns * (BK * 2), mb = c.m0a + (unsigned)(ns & 3) * A_STAGE;
    if constexpr (ISSUE) {
#pragma unroll
        for (int j = 0; j < 4; ++j) dma16(mb + (unsigned)(j * 4096), c.vx, c.srdX, sx + (unsigned)(j * 32 * XROW));
    }
    wait_B<WV>(q);
#pragma unroll
    for (int kk = 0; kk < 4; ++kk) {
        dequant_pair(o, kk, q[2 * kk], q[2 * kk + 1], S, C);
        if (kk == 3 && c.act) {
#pragma unroll
            for (int j = 0; j < 4; ++j) *(LAS u32x4*)(uintptr_t)(((j >> 1) ? c.wa1 : c.wa0) + (unsigned)(j * 128 + bufoff)) = o[j];
        }
        if constexpr (ISSUE) {
            ld_q(q[2 * kk], c.vq, c.srdQ, sq + (unsigned)(2 * kk) * QROW);
            ld_q(q[2 * kk + 1], c.vq, c.srdQ, sq + (unsigned)(2 * kk + 1) * QROW);
        }
    }
    if constexpr (ISSUE) {
#pragma unroll
        for (int j = 4; j < 8; ++j) dma16(mb + (unsigned)(j * 4096), c.vx, c.srdX, sx + (unsigned)(j * 32 * XROW));
    }
}

struct Frag { bf16x8 x[4]; bf16x8 w[7]; };
DI void read_frags(Frag& f, unsigned xa, unsigned w0, unsigned w1, int bufoff) {
    f.x[0] = *(LAS const bf16x8*)(uintptr_t)(xa);
#pragma unroll
    for (int t = 0; t < 7; ++t)
        f.w[t] = *(LAS const bf16x8*)(uintptr_t)(((t & 1) ? w1 : w0) + (unsigned)(t * 2048 + bufoff));
#pragma unroll
    for (int m = 1; m < 4; ++m)
        f.x[m] = *(LAS const bf16x8*)(uintptr_t)(xa + (unsigned)(m * 2048));
}
DI void mma(f32x4 (&acc)[4][7], const Frag& f) {
#pragma unroll
    for (int m = 0; m < 4; ++m)
#pragma unroll
        for (int t = 0; t < 7; ++t)
            acc[m][t] = __builtin_amdgcn_mfma_f32_16x16x32_bf16(f.w[t], f.x[m], acc[m][t], 0, 0, 0);
}
DI void interleave() {
#pragma unroll
    for (int i = 0; i < 11; ++i) {
        __builtin_amdgcn_sched_group_barrier(0x008, 2, 0);
        __builtin_amdgcn_sched_group_barrier(0x100, 1, 0);
    }
    __builtin_amdgcn_sched_group_barrier(0x008, 6, 0);
}

__global__ __launch_bounds__(512, 2)
void w4a16_kernel(const unsigned short* __restrict__ x, const int* __restrict__ qw, const unsigned short* __restrict__ sc,
                  const unsigned short* __restrict__ zr, unsigned short* __restrict__ out, void* ws) {
    __shared__ __attribute__((aligned(1024))) char smem[LDS_TOTAL];
    const unsigned lbase = (unsigned)(uintptr_t)(LAS char*)smem;
    const int lane = threadIdx.x & 63;
    const int wid = __builtin_amdgcn_readfirstlane(threadIdx.x >> 6);
    const int bid = blockIdx.x, xcd = bid & 7, idx = bid >> 3;
    const int tm = idx & 7, tn = xcd * 4 + (idx >> 3);
    const int m0 = tm * BM, n0 = tn * BN;
    STAMP_DECL;
    STAMP(0);
    unsigned long long dg0, dg2, dg19, dg21, dc2, dc19;
#define RT(v) asm volatile("s_memrealtime %0\n\ts_waitcnt lgkmcnt(0)" : "=s"(v) :: "memory")
#define CT(v) asm volatile("s_memtime %0\n\ts_waitcnt lgkmcnt(0)" : "=s"(v) :: "memory")
    RT(dg0);
    if (threadIdx.x == 0) __hip_atomic_store((unsigned long long*)ws + blockIdx.x, dg0, __ATOMIC_RELAXED, __HIP_MEMORY_SCOPE_AGENT);

    if (wid >= 4) {
        const int pw = wid - 4;
        PCtx c;
        c.srdQ = __builtin_amdgcn_make_buffer_rsrc((void*)qw, (short)0, K_ * N_ * 4, 0x00020000);
        c.srdX = __builtin_amdgcn_make_buffer_rsrc((void*)x, (short)0, M_ * K_ * 2, 0x00020000);
        const int kb = 2 * pw + (lane >> 5), nb = lane & 31;
        c.act = nb < 28;
        c.vq = c.act ? (unsigned)(kb * 8 * QROW + (n0 + 4 * nb) * 4) : 0x80000000u;
        const int rin = pw * 8 + (lane >> 3);
        c.vx = (unsigned)((m0 + rin) * XROW + (((lane & 7) ^ ((rin >> 1) & 7)) * 16));
        c.m0a = lbase + LDS_A + pw * 1024;
        u32x4 q0[8], q1[8];
        const unsigned pq = lane < 56 ? (unsigned)((lane / 14) * QROW + xcd * 1792 + (lane % 14) * 128) : 0x80000000u;
        const unsigned pxg = (unsigned)((bid * 8 + (lane & 7)) * XROW + (lane >> 3) * 128);
        const unsigned sink = lbase + LDS_PF;
#pragma unroll
        for (int i = 0; i < 8; ++i) ld_q(q0[i], c.vq, c.srdQ, (unsigned)i * QROW);
#pragma unroll
        for (int j = 0; j < 8; ++j) dma16(c.m0a + (unsigned)(j * 4096), c.vx, c.srdX, (unsigned)(j * 32 * XROW));
        if (PFD) {
            if (pw < 2) dma4(sink, pq, c.srdQ, (unsigned)((pw * 128 + 64 + idx * 4) * QROW));
            else        dma4(sink, pxg, c.srdX, (unsigned)((pw - 2) * 1024));
        }
#pragma unroll
        for (int i = 0; i < 8; ++i) ld_q(q1[i], c.vq, c.srdQ, (unsigned)(BK + i) * QROW);
#pragma unroll
        for (int j = 0; j < 8; ++j) dma16(c.m0a + (unsigned)(A_STAGE + j * 4096), c.vx, c.srdX, (unsigned)(BK * 2 + j * 32 * XROW));
        const int rb = nb < 24 ? (32 * (nb >> 3) + 16 * (nb & 1) + 4 * ((nb >> 1) & 3)) : (96 + 4 * (nb - 24));
        const int f = ((rb >> 1) & 7) ^ ((rb >> 4) & 1);
        c.wa0 = lbase + (unsigned)(LDS_B + rb * 128 + ((kb ^ f) * 16));
        c.wa1 = c.wa0 ^ 16u;
        unsigned sza = lbase + (unsigned)(LDS_SZ + (c.act ? nb : 0) * 16);
        BAR();
        STAMP(1);
        float S[4], C[4];
#define LOAD_SCALES() do { const u32x4 sz = *(LAS const u32x4*)(uintptr_t)sza; sza += BN * 4; \
            _Pragma("unroll") for (int j = 0; j < 4; ++j) { \
                const float s = __uint_as_float(sz[j] << 16), z = __uint_as_float(sz[j] & 0xffff0000u); \
                S[j] = fmul(s, 16777216.0f); C[j] = fmul(-z, s); } } while (0)
#define PF_TURN(b) do { if (PFD && (b) + 5 < NIT && ((b) & 7) == (idx & 7)) dma4(sink, pq, c.srdQ, (unsigned)((((b) + 5) * 64 + (idx >> 3) * 16 + pw * 4) * QROW)); } while (0)
        LOAD_SCALES();
        for (int b = 0; b < NIT - 2; b += 2) {
            pstep<true, 20>(q0, S, C, c, 0, b + 2);
            PF_TURN(b);
            WAIT_L0();
            BAR();
            LSTAMP(2 + b);
            pstep<true, 20>(q1, S, C, c, B_STAGE, b + 3);
            PF_TURN(b + 1);
            LOAD_SCALES();
            WAIT_L0();
            BAR();
            LSTAMP(3 + b);
        }
        pstep<false, 16>(q0, S, C, c, 0, 0);
        WAIT_L0();
        BAR();
        LSTAMP(16);
        pstep<false, 0>(q1, S, C, c, B_STAGE, 0);
        WAIT_L0();
        BAR();
        STAMP(17);
        BAR();
    } else {
        const int w = wid;
        {
            const int t = threadIdx.x;
            if (t < NG * (BN / 4)) {
                const int g = t / (BN / 4), c4 = t % (BN / 4);
                const u32x2 s = *(const u32x2*)(sc + g * N_ + n0 + c4 * 4);
                const u32x2 z = *(const u32x2*)(zr + g * N_ + n0 + c4 * 4);
                u32x4 o;
                o[0] = (s[0] & 0xffffu) | (z[0] << 16);
                o[1] = (s[0] >> 16) | (z[0] & 0xffff0000u);
                o[2] = (s[1] & 0xffffu) | (z[1] << 16);
                o[3] = (s[1] >> 16) | (z[1] & 0xffff0000u);
                *(LAS u32x4*)(uintptr_t)(lbase + LDS_SZ + t * 16) = o;
            }
            asm volatile("" :: "s"(out));
            __builtin_amdgcn_s_waitcnt(0xC07F);
            BAR();
            STAMP(1);
        }
        const int fr = lane & 15, fq = lane >> 4;
        const unsigned sw = (unsigned)((fq ^ (fr >> 1)) * 16);
        unsigned xa0 = lbase + (unsigned)(LDS_A + (w * 64 + fr) * 128) + sw;
        unsigned w00 = lbase + (unsigned)(LDS_B + fr * 128) + sw;
        unsigned w01 = w00 ^ 16u, w10 = w00 ^ 64u, w11 = w00 ^ 80u;
        asm volatile("" : "+v"(w00), "+v"(w01), "+v"(w10), "+v"(w11));
        f32x4 acc[4][7];
#pragma unroll
        for (int m = 0; m < 4; ++m)
#pragma unroll
            for (int t = 0; t < 7; ++t) { acc[m][t] = (f32x4){0.f, 0.f, 0.f, 0.f}; asm volatile("" : "+v"(acc[m][t])); }
        Frag f0, f1;
        BAR();
        STAMP(2);
        RT(dg2); CT(dc2);
        read_frags(f0, xa0, w00, w01, 0);
        for (int it = 0; it < NIT; it += 2) {
            unsigned xe = xa0 + (unsigned)((it & 2) * A_STAGE);
            unsigned xn = xa0 + (unsigned)(((it + 2) & 2) * A_STAGE);
            asm volatile("" : "+v"(xe), "+v"(xn));
            SB0();
            read_frags(f1, xe ^ 64u, w10, w11, 0);
            mma(acc, f0);
            interleave();
            SB0();
            WAIT_L0();
            BAR();
            LSTAMP(3 + it);
            SB0();
            read_frags(f0, xe + A_STAGE, w00, w01, B_STAGE);
            mma(acc, f1);
            interleave();
            SB0();
            read_frags(f1, (xe ^ 64u) + A_STAGE, w10, w11, B_STAGE);
            mma(acc, f0);
            interleave();
            SB0();
            WAIT_L0();
            BAR();
            LSTAMP(4 + it);
            SB0();
            read_frags(f0, xn, w00, w01, 0);
            mma(acc, f1);
            interleave();
            SB0();
        }
        STAMP(19);
        SB0(); RT(dg19); CT(dc19); SB0();
        const unsigned eb = lbase + (unsigned)(A_STAGE + w * (64 * OROW));
        const unsigned ew = eb + (unsigned)(fr * OROW + fq * 16);
#pragma unroll
        for (int m = 0; m < 4; ++m) {
#pragma unroll
            for (int pp = 0; pp < 3; ++pp) {
                u32x4 v;
                v[0] = cvt_pk_bf16(acc[m][2 * pp][0], acc[m][2 * pp][1]);
                v[1] = cvt_pk_bf16(acc[m][2 * pp][2], acc[m][2 * pp][3]);
                v[2] = cvt_pk_bf16(acc[m][2 * pp + 1][0], acc[m][2 * pp + 1][1]);
                v[3] = cvt_pk_bf16(acc[m][2 * pp + 1][2], acc[m][2 * pp + 1][3]);
                *(LAS u32x4*)(uintptr_t)(ew + (unsigned)(m * 16 * OROW + pp * 64)) = v;
            }
            u32x2 v;
            v[0] = cvt_pk_bf16(acc[m][6][0], acc[m][6][1]);
            v[1] = cvt_pk_bf16(acc[m][6][2], acc[m][6][3]);
            *(LAS u32x2*)(uintptr_t)(ew - (unsigned)(fq * 8) + (unsigned)(m * 16 * OROW + 192)) = v;
        }
        char* obase = (char*)(out + (size_t)(m0 + w * 64) * N_ + n0);
#pragma unroll
        for (int i = 0; i < 14; ++i) {
            const int cix = i * 64 + lane;
            const int row = (cix * 4682) >> 16, col = cix - row * 14;
            const u32x4 v = *(LAS const u32x4*)(uintptr_t)(eb + (unsigned)(row * OROW + col * 16));
            *(u32x4*)(obase + (unsigned)(row * (N_ * 2) + col * 16)) = v;
        }
        STAMP(20);
        STAMP(21);
        WAIT_V(0);
        RT(dg21);
        unsigned long long skew = 0;
        if (DIAG == 6 || DIAG == 7) {
            long long mn = 0, mx = 0;
            for (int i = 0; i < 4; ++i) { const long long d = (long long)(__hip_atomic_load((unsigned long long*)ws + i * 64 + lane, __ATOMIC_RELAXED, __HIP_MEMORY_SCOPE_AGENT) - dg0);
                if (d > -5000 && d < 5000) { mn = d < mn ? d : mn; mx = d > mx ? d : mx; } }
            for (int o = 32; o; o >>= 1) { long long a = __shfl_xor(mn, o), b = __shfl_xor(mx, o); mn = a < mn ? a : mn; mx = b > mx ? b : mx; }
            skew = (unsigned long long)__builtin_amdgcn_readfirstlane((int)(DIAG == 6 ? mx - mn : (long long)(dg21 - dg0) - mn));
        }
        { const unsigned long long q = DIAG >= 6 ? skew : DIAG == 1 ? dg2 - dg0 : DIAG == 2 ? dg19 - dg2 : DIAG == 3 ? dg21 - dg19 : DIAG == 4 ? (dc19 - dc2) / 24 : DIAG == 5 ? dg21 - dg0 : 0;
          unsigned long long now; do { __builtin_amdgcn_s_sleep(4); RT(now); } while (now - dg21 < 8 * q); }
    }
    STAMP_OUT();
}
}

extern "C" void kernel_launch(void* const* d_in, const int* in_sizes, int n_in,
                              void* d_out, int out_size, void* d_ws, size_t ws_size,
                              hipStream_t stream) {
    w4a16_kernel<<<256, 512, 0, stream>>>((const unsigned short*)d_in[0], (const int*)d_in[1],
        (const unsigned short*)d_in[2], (const unsigned short*)d_in[3], (unsigned short*)d_out, d_ws);
}
```

```cpp
#include <hip/hip_runtime.h>
#include <hip/hip_bf16.h>

typedef short bf16x8 __attribute__((ext_vector_type(8)));
typedef float f32x4 __attribute__((ext_vector_type(4)));
typedef unsigned u32x4 __attribute__((ext_vector_type(4)));
typedef unsigned u32x2 __attribute__((ext_vector_type(2)));
#define LAS __attribute__((address_space(3)))
#define DI __device__ __forceinline__

#define PFD 1
#define TLBW 0
#define STAUX -1
#define EPI 1

namespace {
constexpr int M_ = 2048, N_ = 3584, K_ = 1024, G_ = 128, NG = K_ / G_;
constexpr int BM = 256, BN = 112, BK = 64, NIT = K_ / BK;
constexpr int A_STAGE = BM * BK * 2;
constexpr int B_STAGE = BN * BK * 2;
constexpr int LDS_A = 0, LDS_B = 4 * A_STAGE, LDS_SZ = LDS_B + 2 * B_STAGE;
constexpr int LDS_PF = LDS_SZ + NG * BN * 4;
constexpr int LDS_TOTAL = LDS_PF + 256;
constexpr int QROW = N_ * 4;
constexpr int XROW = K_ * 2;
constexpr int OROW = 240;

#define STAMP(i)
#define STAMP_DECL
#define STAMP_OUT()
#define LSTAMP(i) STAMP(i)
#define WAIT_V(n) asm volatile("s_waitcnt vmcnt(%0)" ::"n"(n) : "memory")
#define WAIT_L0() asm volatile("s_waitcnt lgkmcnt(0)" ::: "memory")
#define BAR() do { asm volatile("" ::: "memory"); __builtin_amdgcn_s_barrier(); asm volatile("" ::: "memory"); } while (0)
#define SB0() __builtin_amdgcn_sched_barrier(0)

DI unsigned cvt_pk_bf16(float lo, float hi) { unsigned r; asm("v_cvt_pk_bf16_f32 %0, %1, %2" : "=v"(r) : "v"(lo), "v"(hi)); return r; }

DI float fmul(float a, float b) { float r; asm volatile("v_mul_f32 %0, %1, %2" : "=v"(r) : "v"(a), "v"(b)); return r; }
DI void ld_q(u32x4& q, unsigned vq, __amdgpu_buffer_rsrc_t srd, unsigned so) {
    asm volatile("buffer_load_dwordx4 %0, %1, %2, %3 offen" : "=v"(q) : "v"(vq), "s"(srd), "s"(so) : "memory");
}
DI void dma16(unsigned m0v, unsigned v, __amdgpu_buffer_rsrc_t srd, unsigned so) {
    asm volatile("s_mov_b32 m0, %0\n\ts_nop 0\n\tbuffer_load_dwordx4 %1, %2, %3 offen lds" :: "s"(m0v), "v"(v), "s"(srd), "s"(so) : "memory");
}
DI void dma4(unsigned m0v, unsigned v, __amdgpu_buffer_rsrc_t srd, unsigned so) {
    asm volatile("s_mov_b32 m0, %0\n\ts_nop 0\n\tbuffer_load_dword %1, %2, %3 offen lds" :: "s"(m0v), "v"(v), "s"(srd), "s"(so) : "memory");
}
template <int W> DI void wait_B(u32x4 (&q)[8]) {
    asm volatile("s_waitcnt vmcnt(%8)"
                 : "+v"(q[0]), "+v"(q[1]), "+v"(q[2]), "+v"(q[3]), "+v"(q[4]), "+v"(q[5]), "+v"(q[6]), "+v"(q[7]) : "n"(W) : "memory");
}
DI void dequant_pair(u32x4 (&o)[4], int kk, const u32x4& qa, const u32x4& qb, const float (&S)[4], const float (&C)[4]) {
    float t0, t1, t2, t3, t4, t5, t6, t7;
    asm volatile(
        "v_fma_mix_f32 %4, %12, %20, %24 op_sel_hi:[1,0,0]\n\t"
        "v_fma_mix_f32 %5, %16, %20, %24 op_sel_hi:[1,0,0]\n\t"
        "v_fma_mix_f32 %6, %13, %21, %25 op_sel_hi:[1,0,0]\n\t"
        "v_fma_mix_f32 %7, %17, %21, %25 op_sel_hi:[1,0,0]\n\t"
        "v_fma_mix_f32 %8, %14, %22, %26 op_sel_hi:[1,0,0]\n\t"
        "v_fma_mix_f32 %9, %18, %22, %26 op_sel_hi:[1,0,0]\n\t"
        "v_fma_mix_f32 %10, %15, %23, %27 op_sel_hi:[1,0,0]\n\t"
        "v_fma_mix_f32 %11, %19, %23, %27 op_sel_hi:[1,0,0]\n\t"
        "v_cvt_pk_bf16_f32 %0, %4, %5\n\t"
        "v_cvt_pk_bf16_f32 %1, %6, %7\n\t"
        "v_cvt_pk_bf16_f32 %2, %8, %9\n\t"
        "v_cvt_pk_bf16_f32 %3, %10, %11"
        : "=&v"(o[0][kk]), "=&v"(o[1][kk]), "=&v"(o[2][kk]), "=&v"(o[3][kk]),
          "=&v"(t0), "=&v"(t1), "=&v"(t2), "=&v"(t3), "=&v"(t4), "=&v"(t5), "=&v"(t6), "=&v"(t7)
        : "v"(qa[0]), "v"(qa[1]), "v"(qa[2]), "v"(qa[3]), "v"(qb[0]), "v"(qb[1]), "v"(qb[2]), "v"(qb[3]),
          "v"(S[0]), "v"(S[1]), "v"(S[2]), "v"(S[3]), "v"(C[0]), "v"(C[1]), "v"(C[2]), "v"(C[3]));
}
struct PCtx { unsigned vq, vx, m0a, wa0, wa1; __amdgpu_buffer_rsrc_t srdQ, srdX; bool act; };
template <bool ISSUE, int WV>
DI void pstep(u32x4 (&q)[8], const float (&S)[4], const float (&C)[4], const PCtx& c, int bufoff, int ns) {
    u32x4 o[4];
    const unsigned sq = (unsigned)ns * (BK * QROW), sx = (unsigned)ns * (BK * 2), mb = c.m0a + (unsigned)(ns & 3) * A_STAGE;
    if constexpr (ISSUE) {
#pragma unroll
        for (int j = 0; j < 4; ++j) dma16(mb + (unsigned)(j * 4096), c.vx, c.srdX, sx + (unsigned)(j * 32 * XROW));
    }
    wait_B<WV>(q);
#pragma unroll
    for (int kk = 0; kk < 4; ++kk) {
        dequant_pair(o, kk, q[2 * kk], q[2 * kk + 1], S, C);
        if (kk == 3 && c.act) {
#pragma unroll
            for (int j = 0; j < 4; ++j) *(LAS u32x4*)(uintptr_t)(((j >> 1) ? c.wa1 : c.wa0) + (unsigned)(j * 128 + bufoff)) = o[j];
        }
        if constexpr (ISSUE) {
            ld_q(q[2 * kk], c.vq, c.srdQ, sq + (unsigned)(2 * kk) * QROW);
            ld_q(q[2 * kk + 1], c.vq, c.srdQ, sq + (unsigned)(2 * kk + 1) * QROW);
        }
    }
    if constexpr (ISSUE) {
#pragma unroll
        for (int j = 4; j < 8; ++j) dma16(mb + (unsigned)(j * 4096), c.vx, c.srdX, sx + (unsigned)(j * 32 * XROW));
    }
}

struct Frag { bf16x8 x[4]; bf16x8 w[7]; };
DI void read_frags(Frag& f, unsigned xa, unsigned w0, unsigned w1, int bufoff) {
    f.x[0] = *(LAS const bf16x8*)(uintptr_t)(xa);
#pragma unroll
    for (int t = 0; t < 7; ++t)
        f.w[t] = *(LAS const bf16x8*)(uintptr_t)(((t & 1) ? w1 : w0) + (unsigned)(t * 2048 + bufoff));
#pragma unroll
    for (int m = 1; m < 4; ++m)
        f.x[m] = *(LAS const bf16x8*)(uintptr_t)(xa + (unsigned)(m * 2048));
}
DI void mma(f32x4 (&acc)[4][7], const Frag& f) {
#pragma unroll
    for (int m = 0; m < 4; ++m)
#pragma unroll
        for (int t = 0; t < 7; ++t)
            acc[m][t] = __builtin_amdgcn_mfma_f32_16x16x32_bf16(f.w[t], f.x[m], acc[m][t], 0, 0, 0);
}
DI void interleave() {
#pragma unroll
    for (int i = 0; i < 11; ++i) {
        __builtin_amdgcn_sched_group_barrier(0x008, 2, 0);
        __builtin_amdgcn_sched_group_barrier(0x100, 1, 0);
    }
    __builtin_amdgcn_sched_group_barrier(0x008, 6, 0);
}

__global__ __launch_bounds__(512, 2)
void w4a16_kernel(const unsigned short* __restrict__ x, const int* __restrict__ qw, const unsigned short* __restrict__ sc,
                  const unsigned short* __restrict__ zr, unsigned short* __restrict__ out, void* ws) {
    __shared__ __attribute__((aligned(1024))) char smem[LDS_TOTAL];
    const unsigned lbase = (unsigned)(uintptr_t)(LAS char*)smem;
    const int lane = threadIdx.x & 63;
    const int wid = __builtin_amdgcn_readfirstlane(threadIdx.x >> 6);
    const int bid = blockIdx.x, xcd = bid & 7, idx = bid >> 3;
    const int tm = idx & 7, tn = xcd * 4 + (idx >> 3);
    const int m0 = tm * BM, n0 = tn * BN;
    STAMP_DECL;
    STAMP(0);

    if (wid >= 4) {
        const int pw = wid - 4;
        PCtx c;
        c.srdQ = __builtin_amdgcn_make_buffer_rsrc((void*)qw, (short)0, K_ * N_ * 4, 0x00020000);
        c.srdX = __builtin_amdgcn_make_buffer_rsrc((void*)x, (short)0, M_ * K_ * 2, 0x00020000);
        const int kb = 2 * pw + (lane >> 5), nb = lane & 31;
        c.act = nb < 28;
        c.vq = c.act ? (unsigned)(kb * 8 * QROW + (n0 + 4 * nb) * 4) : 0x80000000u;
        const int rin = pw * 8 + (lane >> 3);
        c.vx = (unsigned)((m0 + rin) * XROW + (((lane & 7) ^ ((rin >> 1) & 7)) * 16));
        c.m0a = lbase + LDS_A + pw * 1024;
        u32x4 q0[8], q1[8];
        const unsigned pq = lane < 56 ? (unsigned)((lane / 14) * QROW + xcd * 1792 + (lane % 14) * 128) : 0x80000000u;
        const unsigned pxg = (unsigned)((bid * 8 + (lane & 7)) * XROW + (lane >> 3) * 128);
        const unsigned sink = lbase + LDS_PF;
#pragma unroll
        for (int i = 0; i < 8; ++i) ld_q(q0[i], c.vq, c.srdQ, (unsigned)i * QROW);
#pragma unroll
        for (int j = 0; j < 8; ++j) dma16(c.m0a + (unsigned)(j * 4096), c.vx, c.srdX, (unsigned)(j * 32 * XROW));
        if (PFD) {
            if (pw < 2) dma4(sink, pq, c.srdQ, (unsigned)((pw * 128 + 64 + idx * 4) * QROW));
            else        dma4(sink, pxg, c.srdX, (unsigned)((pw - 2) * 1024));
        }
#pragma unroll
        for (int i = 0; i < 8; ++i) ld_q(q1[i], c.vq, c.srdQ, (unsigned)(BK + i) * QROW);
#pragma unroll
        for (int j = 0; j < 8; ++j) dma16(c.m0a + (unsigned)(A_STAGE + j * 4096), c.vx, c.srdX, (unsigned)(BK * 2 + j * 32 * XROW));
        const int rb = nb < 24 ? (32 * (nb >> 3) + 16 * (nb & 1) + 4 * ((nb >> 1) & 3)) : (96 + 4 * (nb - 24));
        const int f = ((rb >> 1) & 7) ^ ((rb >> 4) & 1);
        c.wa0 = lbase + (unsigned)(LDS_B + rb * 128 + ((kb ^ f) * 16));
        c.wa1 = c.wa0 ^ 16u;
        unsigned sza = lbase + (unsigned)(LDS_SZ + (c.act ? nb : 0) * 16);
        BAR();
        STAMP(1);
        float S[4], C[4];
#define LOAD_SCALES() do { const u32x4 sz = *(LAS const u32x4*)(uintptr_t)sza; sza += BN * 4; \
            _Pragma("unroll") for (int j = 0; j < 4; ++j) { \
                const float s = __uint_as_float(sz[j] << 16), z = __uint_as_float(sz[j] & 0xffff0000u); \
                S[j] = fmul(s, 16777216.0f); C[j] = fmul(-z, s); } } while (0)
#define PF_TURN(b) do { if (PFD && (b) + 5 < NIT && ((b) & 7) == (idx & 7)) dma4(sink, pq, c.srdQ, (unsigned)((((b) + 5) * 64 + (idx >> 3) * 16 + pw * 4) * QROW)); } while (0)
        LOAD_SCALES();
        for (int b = 0; b < NIT - 2; b += 2) {
            pstep<true, 20>(q0, S, C, c, 0, b + 2);
            PF_TURN(b);
            WAIT_L0();
            BAR();
            LSTAMP(2 + b);
            pstep<true, 20>(q1, S, C, c, B_STAGE, b + 3);
            PF_TURN(b + 1);
            LOAD_SCALES();
            WAIT_L0();
            BAR();
            LSTAMP(3 + b);
        }
        pstep<false, 16>(q0, S, C, c, 0, 0);
        WAIT_L0();
        BAR();
        LSTAMP(16);
        pstep<false, 0>(q1, S, C, c, B_STAGE, 0);
        WAIT_L0();
        BAR();
        STAMP(17);
        BAR();
    } else {
        const int w = wid;
        {
            const int t = threadIdx.x;
            if (t < NG * (BN / 4)) {
                const int g = t / (BN / 4), c4 = t % (BN / 4);
                const u32x2 s = *(const u32x2*)(sc + g * N_ + n0 + c4 * 4);
                const u32x2 z = *(const u32x2*)(zr + g * N_ + n0 + c4 * 4);
                u32x4 o;
                o[0] = (s[0] & 0xffffu) | (z[0] << 16);
                o[1] = (s[0] >> 16) | (z[0] & 0xffff0000u);
                o[2] = (s[1] & 0xffffu) | (z[1] << 16);
                o[3] = (s[1] >> 16) | (z[1] & 0xffff0000u);
                *(LAS u32x4*)(uintptr_t)(lbase + LDS_SZ + t * 16) = o;
            }
            asm volatile("" :: "s"(out));
            __builtin_amdgcn_s_waitcnt(0xC07F);
            BAR();
            STAMP(1);
        }
        const int fr = lane & 15, fq = lane >> 4;
        const unsigned sw = (unsigned)((fq ^ (fr >> 1)) * 16);
        unsigned xa0 = lbase + (unsigned)(LDS_A + (w * 64 + fr) * 128) + sw;
        unsigned w00 = lbase + (unsigned)(LDS_B + fr * 128) + sw;
        unsigned w01 = w00 ^ 16u, w10 = w00 ^ 64u, w11 = w00 ^ 80u;
        asm volatile("" : "+v"(w00), "+v"(w01), "+v"(w10), "+v"(w11));
        f32x4 acc[4][7];
#pragma unroll
        for (int m = 0; m < 4; ++m)
#pragma unroll
            for (int t = 0; t < 7; ++t) { acc[m][t] = (f32x4){0.f, 0.f, 0.f, 0.f}; asm volatile("" : "+v"(acc[m][t])); }
        Frag f0, f1;
        BAR();
        STAMP(2);
        read_frags(f0, xa0, w00, w01, 0);
        for (int it = 0; it < NIT; it += 2) {
            unsigned xe = xa0 + (unsigned)((it & 2) * A_STAGE);
            unsigned xn = xa0 + (unsigned)(((it + 2) & 2) * A_STAGE);
            asm volatile("" : "+v"(xe), "+v"(xn));
            SB0();
            read_frags(f1, xe ^ 64u, w10, w11, 0);
            mma(acc, f0);
            interleave();
            SB0();
            WAIT_L0();
            BAR();
            LSTAMP(3 + it);
            SB0();
            read_frags(f0, xe + A_STAGE, w00, w01, B_STAGE);
            mma(acc, f1);
            interleave();
            SB0();
            read_frags(f1, (xe ^ 64u) + A_STAGE, w10, w11, B_STAGE);
            mma(acc, f0);
            interleave();
            SB0();
            WAIT_L0();
            BAR();
            LSTAMP(4 + it);
            SB0();
            read_frags(f0, xn, w00, w01, 0);
            mma(acc, f1);
            interleave();
            SB0();
        }
        STAMP(19);
        const unsigned eb = lbase + (unsigned)(A_STAGE + w * (64 * OROW));
        const unsigned ew = eb + (unsigned)(fr * OROW + fq * 16);
#pragma unroll
        for (int m = 0; m < 4; ++m) {
#pragma unroll
            for (int pp = 0; pp < 3; ++pp) {
                u32x4 v;
                v[0] = cvt_pk_bf16(acc[m][2 * pp][0], acc[m][2 * pp][1]);
                v[1] = cvt_pk_bf16(acc[m][2 * pp][2], acc[m][2 * pp][3]);
                v[2] = cvt_pk_bf16(acc[m][2 * pp + 1][0], acc[m][2 * pp + 1][1]);
                v[3] = cvt_pk_bf16(acc[m][2 * pp + 1][2], acc[m][2 * pp + 1][3]);
                *(LAS u32x4*)(uintptr_t)(ew + (unsigned)(m * 16 * OROW + pp * 64)) = v;
            }
            u32x2 v;
            v[0] = cvt_pk_bf16(acc[m][6][0], acc[m][6][1]);
            v[1] = cvt_pk_bf16(acc[m][6][2], acc[m][6][3]);
            *(LAS u32x2*)(uintptr_t)(ew - (unsigned)(fq * 8) + (unsigned)(m * 16 * OROW + 192)) = v;
        }
        char* obase = (char*)(out + (size_t)(m0 + w * 64) * N_ + n0);
        const __amdgpu_buffer_rsrc_t srdO = __builtin_amdgcn_make_buffer_rsrc((void*)out, (short)0, M_ * N_ * 2, 0x00020000);
        const int osoff = ((m0 + w * 64) * N_ + n0) * 2;
#pragma unroll
        for (int i = 0; i < 14; ++i) {
            const int cix = i * 64 + lane;
            const int row = (cix * 4682) >> 16, col = cix - row * 14;
            const u32x4 v = *(LAS const u32x4*)(uintptr_t)(eb + (unsigned)(row * OROW + col * 16));
            *(u32x4*)(obase + (unsigned)(row * (N_ * 2) + col * 16)) = v;
        }
        STAMP(20);
        STAMP(21);
    }
    STAMP_OUT();
}
}

extern "C" void kernel_launch(void* const* d_in, const int* in_sizes, int n_in,
                              void* d_out, int out_size, void* d_ws, size_t ws_size,
                              hipStream_t stream) {
    w4a16_kernel<<<256, 512, 0, stream>>>((const unsigned short*)d_in[0], (const int*)d_in[1],
        (const unsigned short*)d_in[2], (const unsigned short*)d_in[3], (unsigned short*)d_out, d_ws);
}
```

```cpp
#include <hip/hip_runtime.h>
#include <hip/hip_bf16.h>

typedef short bf16x8 __attribute__((ext_vector_type(8)));
typedef float f32x4 __attribute__((ext_vector_type(4)));
typedef unsigned u32x4 __attribute__((ext_vector_type(4)));
typedef unsigned u32x2 __attribute__((ext_vector_type(2)));
#define LAS __attribute__((address_space(3)))
#define DI __device__ __forceinline__

#define PFD 1
#define TLBW 0
#define STAUX 16
#define EPI 1

namespace {
constexpr int M_ = 2048, N_ = 3584, K_ = 1024, G_ = 128, NG = K_ / G_;
constexpr int BM = 256, BN = 112, BK = 64, NIT = K_ / BK;
constexpr int A_STAGE = BM * BK * 2;
constexpr int B_STAGE = BN * BK * 2;
constexpr int LDS_A = 0, LDS_B = 4 * A_STAGE, LDS_SZ = LDS_B + 2 * B_STAGE;
constexpr int LDS_PF = LDS_SZ + NG * BN * 4;
constexpr int LDS_TOTAL = LDS_PF + 256;
constexpr int QROW = N_ * 4;
constexpr int XROW = K_ * 2;
constexpr int OROW = 240;

#define STAMP(i)
#define STAMP_DECL
#define STAMP_OUT()
#define LSTAMP(i) STAMP(i)
#define WAIT_V(n) asm volatile("s_waitcnt vmcnt(%0)" ::"n"(n) : "memory")
#define WAIT_L0() asm volatile("s_waitcnt lgkmcnt(0)" ::: "memory")
#define BAR() do { asm volatile("" ::: "memory"); __builtin_amdgcn_s_barrier(); asm volatile("" ::: "memory"); } while (0)
#define SB0() __builtin_amdgcn_sched_barrier(0)

DI unsigned cvt_pk_bf16(float lo, float hi) { unsigned r; asm("v_cvt_pk_bf16_f32 %0, %1, %2" : "=v"(r) : "v"(lo), "v"(hi)); return r; }

DI float fmul(float a, float b) { float r; asm volatile("v_mul_f32 %0, %1, %2" : "=v"(r) : "v"(a), "v"(b)); return r; }
DI void ld_q(u32x4& q, unsigned vq, __amdgpu_buffer_rsrc_t srd, unsigned so) {
    asm volatile("buffer_load_dwordx4 %0, %1, %2, %3 offen" : "=v"(q) : "v"(vq), "s"(srd), "s"(so) : "memory");
}
DI void dma16(unsigned m0v, unsigned v, __amdgpu_buffer_rsrc_t srd, unsigned so) {
    asm volatile("s_mov_b32 m0, %0\n\ts_nop 0\n\tbuffer_load_dwordx4 %1, %2, %3 offen lds" :: "s"(m0v), "v"(v), "s"(srd), "s"(so) : "memory");
}
DI void dma4(unsigned m0v, unsigned v, __amdgpu_buffer_rsrc_t srd, unsigned so) {
    asm volatile("s_mov_b32 m0, %0\n\ts_nop 0\n\tbuffer_load_dword %1, %2, %3 offen lds" :: "s"(m0v), "v"(v), "s"(srd), "s"(so) : "memory");
}
template <int W> DI void wait_B(u32x4 (&q)[8]) {
    asm volatile("s_waitcnt vmcnt(%8)"
                 : "+v"(q[0]), "+v"(q[1]), "+v"(q[2]), "+v"(q[3]), "+v"(q[4]), "+v"(q[5]), "+v"(q[6]), "+v"(q[7]) : "n"(W) : "memory");
}
DI void dequant_pair(u32x4 (&o)[4], int kk, const u32x4& qa, const u32x4& qb, const float (&S)[4], const float (&C)[4]) {
    float t0, t1, t2, t3, t4, t5, t6, t7;
    asm volatile(
        "v_fma_mix_f32 %4, %12, %20, %24 op_sel_hi:[1,0,0]\n\t"
        "v_fma_mix_f32 %5, %16, %20, %24 op_sel_hi:[1,0,0]\n\t"
        "v_fma_mix_f32 %6, %13, %21, %25 op_sel_hi:[1,0,0]\n\t"
        "v_fma_mix_f32 %7, %17, %21, %25 op_sel_hi:[1,0,0]\n\t"
        "v_fma_mix_f32 %8, %14, %22, %26 op_sel_hi:[1,0,0]\n\t"
        "v_fma_mix_f32 %9, %18, %22, %26 op_sel_hi:[1,0,0]\n\t"
        "v_fma_mix_f32 %10, %15, %23, %27 op_sel_hi:[1,0,0]\n\t"
        "v_fma_mix_f32 %11, %19, %23, %27 op_sel_hi:[1,0,0]\n\t"
        "v_cvt_pk_bf16_f32 %0, %4, %5\n\t"
        "v_cvt_pk_bf16_f32 %1, %6, %7\n\t"
        "v_cvt_pk_bf16_f32 %2, %8, %9\n\t"
        "v_cvt_pk_bf16_f32 %3, %10, %11"
        : "=&v"(o[0][kk]), "=&v"(o[1][kk]), "=&v"(o[2][kk]), "=&v"(o[3][kk]),
          "=&v"(t0), "=&v"(t1), "=&v"(t2), "=&v"(t3), "=&v"(t4), "=&v"(t5), "=&v"(t6), "=&v"(t7)
        : "v"(qa[0]), "v"(qa[1]), "v"(qa[2]), "v"(qa[3]), "v"(qb[0]), "v"(qb[1]), "v"(qb[2]), "v"(qb[3]),
          "v"(S[0]), "v"(S[1]), "v"(S[2]), "v"(S[3]), "v"(C[0]), "v"(C[1]), "v"(C[2]), "v"(C[3]));
}
struct PCtx { unsigned vq, vx, m0a, wa0, wa1; __amdgpu_buffer_rsrc_t srdQ, srdX; bool act; };
template <bool ISSUE, int WV>
DI void pstep(u32x4 (&q)[8], const float (&S)[4], const float (&C)[4], const PCtx& c, int bufoff, int ns) {
    u32x4 o[4];
    const unsigned sq = (unsigned)ns * (BK * QROW), sx = (unsigned)ns * (BK * 2), mb = c.m0a + (unsigned)(ns & 3) * A_STAGE;
    if constexpr (ISSUE) {
#pragma unroll
        for (int j = 0; j < 4; ++j) dma16(mb + (unsigned)(j * 4096), c.vx, c.srdX, sx + (unsigned)(j * 32 * XROW));
    }
    wait_B<WV>(q);
#pragma unroll
    for (int kk = 0; kk < 4; ++kk) {
        dequant_pair(o, kk, q[2 * kk], q[2 * kk + 1], S, C);
        if (kk == 3 && c.act) {
#pragma unroll
            for (int j = 0; j < 4; ++j) *(LAS u32x4*)(uintptr_t)(((j >> 1) ? c.wa1 : c.wa0) + (unsigned)(j * 128 + bufoff)) = o[j];
        }
        if constexpr (ISSUE) {
            ld_q(q[2 * kk], c.vq, c.srdQ, sq + (unsigned)(2 * kk) * QROW);
            ld_q(q[2 * kk + 1], c.vq, c.srdQ, sq + (unsigned)(2 * kk + 1) * QROW);
        }
    }
    if constexpr (ISSUE) {
#pragma unroll
        for (int j = 4; j < 8; ++j) dma16(mb + (unsigned)(j * 4096), c.vx, c.srdX, sx + (unsigned)(j * 32 * XROW));
    }
}

struct Frag { bf16x8 x[4]; bf16x8 w[7]; };
DI void read_frags(Frag& f, unsigned xa, unsigned w0, unsigned w1, int bufoff) {
    f.x[0] = *(LAS const bf16x8*)(uintptr_t)(xa);
#pragma unroll
    for (int t = 0; t < 7; ++t)
        f.w[t] = *(LAS const bf16x8*)(uintptr_t)(((t & 1) ? w1 : w0) + (unsigned)(t * 2048 + bufoff));
#pragma unroll
    for (int m = 1; m < 4; ++m)
        f.x[m] = *(LAS const bf16x8*)(uintptr_t)(xa + (unsigned)(m * 2048));
}
DI void mma(f32x4 (&acc)[4][7], const Frag& f) {
#pragma unroll
    for (int m = 0; m < 4; ++m)
#pragma unroll
        for (int t = 0; t < 7; ++t)
            acc[m][t] = __builtin_amdgcn_mfma_f32_16x16x32_bf16(f.w[t], f.x[m], acc[m][t], 0, 0, 0);
}
DI void interleave() {
#pragma unroll
    for (int i = 0; i < 11; ++i) {
        __builtin_amdgcn_sched_group_barrier(0x008, 2, 0);
        __builtin_amdgcn_sched_group_barrier(0x100, 1, 0);
    }
    __builtin_amdgcn_sched_group_barrier(0x008, 6, 0);
}

__global__ __launch_bounds__(512, 2)
void w4a16_kernel(const unsigned short* __restrict__ x, const int* __restrict__ qw, const unsigned short* __restrict__ sc,
                  const unsigned short* __restrict__ zr, unsigned short* __restrict__ out, void* ws) {
    __shared__ __attribute__((aligned(1024))) char smem[LDS_TOTAL];
    const unsigned lbase = (unsigned)(uintptr_t)(LAS char*)smem;
    const int lane = threadIdx.x & 63;
    const int wid = __builtin_amdgcn_readfirstlane(threadIdx.x >> 6);
    const int bid = blockIdx.x, xcd = bid & 7, idx = bid >> 3;
    const int tm = idx & 7, tn = xcd * 4 + (idx >> 3);
    const int m0 = tm * BM, n0 = tn * BN;
    STAMP_DECL;
    STAMP(0);
    unsigned long long dg0;
#define RT(v) asm volatile("s_memrealtime %0\n\ts_waitcnt lgkmcnt(0)" : "=s"(v) :: "memory")
    RT(dg0);

    if (wid >= 4) {
        const int pw = wid - 4;
        PCtx c;
        c.srdQ = __builtin_amdgcn_make_buffer_rsrc((void*)qw, (short)0, K_ * N_ * 4, 0x00020000);
        c.srdX = __builtin_amdgcn_make_buffer_rsrc((void*)x, (short)0, M_ * K_ * 2, 0x00020000);
        const int kb = 2 * pw + (lane >> 5), nb = lane & 31;
        c.act = nb < 28;
        c.vq = c.act ? (unsigned)(kb * 8 * QROW + (n0 + 4 * nb) * 4) : 0x80000000u;
        const int rin = pw * 8 + (lane >> 3);
        c.vx = (unsigned)((m0 + rin) * XROW + (((lane & 7) ^ ((rin >> 1) & 7)) * 16));
        c.m0a = lbase + LDS_A + pw * 1024;
        u32x4 q0[8], q1[8];
        const unsigned pq = lane < 56 ? (unsigned)((lane / 14) * QROW + xcd * 1792 + (lane % 14) * 128) : 0x80000000u;
        const unsigned pxg = (unsigned)((bid * 8 + (lane & 7)) * XROW + (lane >> 3) * 128);
        const unsigned sink = lbase + LDS_PF;
#pragma unroll
        for (int i = 0; i < 8; ++i) ld_q(q0[i], c.vq, c.srdQ, (unsigned)i * QROW);
#pragma unroll
        for (int j = 0; j < 8; ++j) dma16(c.m0a + (unsigned)(j * 4096), c.vx, c.srdX, (unsigned)(j * 32 * XROW));
        if (PFD) {
            if (pw < 2) dma4(sink, pq, c.srdQ, (unsigned)((pw * 128 + 64 + idx * 4) * QROW));
            else        dma4(sink, pxg, c.srdX, (unsigned)((pw - 2) * 1024));
        }
#pragma unroll
        for (int i = 0; i < 8; ++i) ld_q(q1[i], c.vq, c.srdQ, (unsigned)(BK + i) * QROW);
#pragma unroll
        for (int j = 0; j < 8; ++j) dma16(c.m0a + (unsigned)(A_STAGE + j * 4096), c.vx, c.srdX, (unsigned)(BK * 2 + j * 32 * XROW));
        const int rb = nb < 24 ? (32 * (nb >> 3) + 16 * (nb & 1) + 4 * ((nb >> 1) & 3)) : (96 + 4 * (nb - 24));
        const int f = ((rb >> 1) & 7) ^ ((rb >> 4) & 1);
        c.wa0 = lbase + (unsigned)(LDS_B + rb * 128 + ((kb ^ f) * 16));
        c.wa1 = c.wa0 ^ 16u;
        unsigned sza = lbase + (unsigned)(LDS_SZ + (c.act ? nb : 0) * 16);
        BAR();
        STAMP(1);
        float S[4], C[4];
#define LOAD_SCALES() do { const u32x4 sz = *(LAS const u32x4*)(uintptr_t)sza; sza += BN * 4; \
            _Pragma("unroll") for (int j = 0; j < 4; ++j) { \
                const float s = __uint_as_float(sz[j] << 16), z = __uint_as_float(sz[j] & 0xffff0000u); \
                S[j] = fmul(s, 16777216.0f); C[j] = fmul(-z, s); } } while (0)
#define PF_TURN(b) do { if (PFD && (b) + 5 < NIT && ((b) & 7) == (idx & 7)) dma4(sink, pq, c.srdQ, (unsigned)((((b) + 5) * 64 + (idx >> 3) * 16 + pw * 4) * QROW)); } while (0)
        LOAD_SCALES();
        for (int b = 0; b < NIT - 2; b += 2) {
            pstep<true, 20>(q0, S, C, c, 0, b + 2);
            PF_TURN(b);
            WAIT_L0();
            BAR();
            LSTAMP(2 + b);
            pstep<true, 20>(q1, S, C, c, B_STAGE, b + 3);
            PF_TURN(b + 1);
            LOAD_SCALES();
            WAIT_L0();
            BAR();
            LSTAMP(3 + b);
        }
        pstep<false, 16>(q0, S, C, c, 0, 0);
        WAIT_L0();
        BAR();
        LSTAMP(16);
        pstep<false, 0>(q1, S, C, c, B_STAGE, 0);
        WAIT_L0();
        BAR();
        STAMP(17);
        BAR();
    } else {
        const int w = wid;
        {
            const int t = threadIdx.x;
            if (t < NG * (BN / 4)) {
                const int g = t / (BN / 4), c4 = t % (BN / 4);
                const u32x2 s = *(const u32x2*)(sc + g * N_ + n0 + c4 * 4);
                const u32x2 z = *(const u32x2*)(zr + g * N_ + n0 + c4 * 4);
                u32x4 o;
                o[0] = (s[0] & 0xffffu) | (z[0] << 16);
                o[1] = (s[0] >> 16) | (z[0] & 0xffff0000u);
                o[2] = (s[1] & 0xffffu) | (z[1] << 16);
                o[3] = (s[1] >> 16) | (z[1] & 0xffff0000u);
                *(LAS u32x4*)(uintptr_t)(lbase + LDS_SZ + t * 16) = o;
            }
            asm volatile("" :: "s"(out));
            __builtin_amdgcn_s_waitcnt(0xC07F);
            BAR();
            STAMP(1);
        }
        const int fr = lane & 15, fq = lane >> 4;
        const unsigned sw = (unsigned)((fq ^ (fr >> 1)) * 16);
        unsigned xa0 = lbase + (unsigned)(LDS_A + (w * 64 + fr) * 128) + sw;
        unsigned w00 = lbase + (unsigned)(LDS_B + fr * 128) + sw;
        unsigned w01 = w00 ^ 16u, w10 = w00 ^ 64u, w11 = w00 ^ 80u;
        asm volatile("" : "+v"(w00), "+v"(w01), "+v"(w10), "+v"(w11));
        f32x4 acc[4][7];
#pragma unroll
        for (int m = 0; m < 4; ++m)
#pragma unroll
            for (int t = 0; t < 7; ++t) { acc[m][t] = (f32x4){0.f, 0.f, 0.f, 0.f}; asm volatile("" : "+v"(acc[m][t])); }
        Frag f0, f1;
        BAR();
        STAMP(2);
        read_frags(f0, xa0, w00, w01, 0);
        for (int it = 0; it < NIT; it += 2) {
            unsigned xe = xa0 + (unsigned)((it & 2) * A_STAGE);
            unsigned xn = xa0 + (unsigned)(((it + 2) & 2) * A_STAGE);
            asm volatile("" : "+v"(xe), "+v"(xn));
            SB0();
            read_frags(f1, xe ^ 64u, w10, w11, 0);
            mma(acc, f0);
            interleave();
            SB0();
            WAIT_L0();
            BAR();
            LSTAMP(3 + it);
            SB0();
            read_frags(f0, xe + A_STAGE, w00, w01, B_STAGE);
            mma(acc, f1);
            interleave();
            SB0();
            read_frags(f1, (xe ^ 64u) + A_STAGE, w10, w11, B_STAGE);
            mma(acc, f0);
            interleave();
            SB0();
            WAIT_L0();
            BAR();
            LSTAMP(4 + it);
            SB0();
            read_frags(f0, xn, w00, w01, 0);
            mma(acc, f1);
            interleave();
            SB0();
        }
        STAMP(19);
        const unsigned eb = lbase + (unsigned)(A_STAGE + w * (64 * OROW));
        const unsigned ew = eb + (unsigned)(fr * OROW + fq * 16);
#pragma unroll
        for (int m = 0; m < 4; ++m) {
#pragma unroll
            for (int pp = 0; pp < 3; ++pp) {
                u32x4 v;
                v[0] = cvt_pk_bf16(acc[m][2 * pp][0], acc[m][2 * pp][1]);
                v[1] = cvt_pk_bf16(acc[m][2 * pp][2], acc[m][2 * pp][3]);
                v[2] = cvt_pk_bf16(acc[m][2 * pp + 1][0], acc[m][2 * pp + 1][1]);
                v[3] = cvt_pk_bf16(acc[m][2 * pp + 1][2], acc[m][2 * pp + 1][3]);
                *(LAS u32x4*)(uintptr_t)(ew + (unsigned)(m * 16 * OROW + pp * 64)) = v;
            }
            u32x2 v;
            v[0] = cvt_pk_bf16(acc[m][6][0], acc[m][6][1]);
            v[1] = cvt_pk_bf16(acc[m][6][2], acc[m][6][3]);
            *(LAS u32x2*)(uintptr_t)(ew - (unsigned)(fq * 8) + (unsigned)(m * 16 * OROW + 192)) = v;
        }
        char* obase = (char*)(out + (size_t)(m0 + w * 64) * N_ + n0);
        const __amdgpu_buffer_rsrc_t srdO = __builtin_amdgcn_make_buffer_rsrc((void*)out, (short)0, M_ * N_ * 2, 0x00020000);
        const int osoff = ((m0 + w * 64) * N_ + n0) * 2;
#pragma unroll
        for (int i = 0; i < 14; ++i) {
            const int cix = i * 64 + lane;
            const int row = (cix * 4682) >> 16, col = cix - row * 14;
            const u32x4 v = *(LAS const u32x4*)(uintptr_t)(eb + (unsigned)(row * OROW + col * 16));
            __builtin_amdgcn_raw_buffer_store_b128(v, srdO, (int)(row * (N_ * 2) + col * 16), osoff, STAUX);
        }
        STAMP(20);
        STAMP(21);
        WAIT_V(0);
        { unsigned long long now; do { __builtin_amdgcn_s_sleep(2); RT(now); } while (now - dg0 < 4000); }
    }
    STAMP_OUT();
}
}

extern "C" void kernel_launch(void* const* d_in, const int* in_sizes, int n_in,
                              void* d_out, int out_size, void* d_ws, size_t ws_size,
                              hipStream_t stream) {
    w4a16_kernel<<<256, 512, 0, stream>>>((const unsigned short*)d_in[0], (const int*)d_in[1],
        (const unsigned short*)d_in[2], (const unsigned short*)d_in[3], (unsigned short*)d_out, d_ws);
}
```

```cpp
#include <hip/hip_runtime.h>
#include <hip/hip_bf16.h>

typedef short bf16x8 __attribute__((ext_vector_type(8)));
typedef float f32x4 __attribute__((ext_vector_type(4)));
typedef unsigned u32x4 __attribute__((ext_vector_type(4)));
typedef unsigned u32x2 __attribute__((ext_vector_type(2)));
#define LAS __attribute__((address_space(3)))
#define DI __device__ __forceinline__

#define QFIRST 4
#define HELP 1
#define HSTART 1
#define PFX 1
#define XROT 1
#define STAUX 2

namespace {
constexpr int M_ = 2048, N_ = 3584, K_ = 1024;
constexpr int BM = 256, BN = 128, BK = 64, NIT = K_ / BK;
constexpr int A_STAGE = BM * BK * 2;
constexpr int B_STAGE = BN * BK * 2;
constexpr int LDS_A = 0, LDS_B = 4 * A_STAGE;
constexpr int LDS_TOTAL = LDS_B + 2 * B_STAGE;
constexpr int QROW = N_ * 4;
constexpr int XROW = K_ * 2;
constexpr int OROW = 272;

#define STAMP(i)
#define STAMP_DECL
#define STAMP_OUT()
#define LSTAMP(i) STAMP(i)
#define WAIT_V(n) asm volatile("s_waitcnt vmcnt(%0)" ::"n"(n) : "memory")
#define WAIT_L0() asm volatile("s_waitcnt lgkmcnt(0)" ::: "memory")
#define BAR() do { asm volatile("" ::: "memory"); __builtin_amdgcn_s_barrier(); asm volatile("" ::: "memory"); } while (0)
#define SB0() __builtin_amdgcn_sched_barrier(0)

DI unsigned cvt_pk_bf16(float lo, float hi) { unsigned r; asm("v_cvt_pk_bf16_f32 %0, %1, %2" : "=v"(r) : "v"(lo), "v"(hi)); return r; }
DI float fmul(float a, float b) { float r; asm volatile("v_mul_f32 %0, %1, %2" : "=v"(r) : "v"(a), "v"(b)); return r; }
DI void ld_q(u32x4& q, unsigned vq, __amdgpu_buffer_rsrc_t srd, unsigned so) {
    asm volatile("buffer_load_dwordx4 %0, %1, %2, %3 offen" : "=v"(q) : "v"(vq), "s"(srd), "s"(so) : "memory");
}
DI void ld8(u32x2& q, unsigned v, __amdgpu_buffer_rsrc_t srd, unsigned so) {
    asm volatile("buffer_load_dwordx2 %0, %1, %2, %3 offen" : "=v"(q) : "v"(v), "s"(srd), "s"(so) : "memory");
}
DI void dma16(unsigned m0v, unsigned v, __amdgpu_buffer_rsrc_t srd, unsigned so) {
    asm volatile("s_mov_b32 m0, %0\n\ts_nop 0\n\tbuffer_load_dwordx4 %1, %2, %3 offen lds" :: "s"(m0v), "v"(v), "s"(srd), "s"(so) : "memory");
}
DI void dma4(unsigned m0v, unsigned v, __amdgpu_buffer_rsrc_t srd, unsigned so) {
    asm volatile("s_mov_b32 m0, %0\n\ts_nop 0\n\tbuffer_load_dword %1, %2, %3 offen lds" :: "s"(m0v), "v"(v), "s"(srd), "s"(so) : "memory");
}
template <int W> DI void wait_q(u32x4 (&q)[8]) {
    asm volatile("s_waitcnt vmcnt(%8)"
                 : "+v"(q[0]), "+v"(q[1]), "+v"(q[2]), "+v"(q[3]), "+v"(q[4]), "+v"(q[5]), "+v"(q[6]), "+v"(q[7]) : "n"(W) : "memory");
}
DI void dequant_pair(u32x4 (&o)[4], int kk, const u32x4& qa, const u32x4& qb, const float (&S)[4], const float (&C)[4]) {
    float t0, t1, t2, t3, t4, t5, t6, t7;
    asm volatile(
        "v_fma_mix_f32 %4, %12, %20, %24 op_sel_hi:[1,0,0]\n\t"
        "v_fma_mix_f32 %5, %16, %20, %24 op_sel_hi:[1,0,0]\n\t"
        "v_fma_mix_f32 %6, %13, %21, %25 op_sel_hi:[1,0,0]\n\t"
        "v_fma_mix_f32 %7, %17, %21, %25 op_sel_hi:[1,0,0]\n\t"
        "v_fma_mix_f32 %8, %14, %22, %26 op_sel_hi:[1,0,0]\n\t"
        "v_fma_mix_f32 %9, %18, %22, %26 op_sel_hi:[1,0,0]\n\t"
        "v_fma_mix_f32 %10, %15, %23, %27 op_sel_hi:[1,0,0]\n\t"
        "v_fma_mix_f32 %11, %19, %23, %27 op_sel_hi:[1,0,0]\n\t"
        "v_cvt_pk_bf16_f32 %0, %4, %5\n\t"
        "v_cvt_pk_bf16_f32 %1, %6, %7\n\t"
        "v_cvt_pk_bf16_f32 %2, %8, %9\n\t"
        "v_cvt_pk_bf16_f32 %3, %10, %11"
        : "=&v"(o[0][kk]), "=&v"(o[1][kk]), "=&v"(o[2][kk]), "=&v"(o[3][kk]),
          "=&v"(t0), "=&v"(t1), "=&v"(t2), "=&v"(t3), "=&v"(t4), "=&v"(t5), "=&v"(t6), "=&v"(t7)
        : "v"(qa[0]), "v"(qa[1]), "v"(qa[2]), "v"(qa[3]), "v"(qb[0]), "v"(qb[1]), "v"(qb[2]), "v"(qb[3]),
          "v"(S[0]), "v"(S[1]), "v"(S[2]), "v"(S[3]), "v"(C[0]), "v"(C[1]), "v"(C[2]), "v"(C[3]));
}
DI void make_scales(float (&S)[4], float (&C)[4], const u32x2& s2, const u32x2& z2) {
#pragma unroll
    for (int j = 0; j < 4; ++j) {
        const float s = __uint_as_float((j & 1) ? (s2[j >> 1] & 0xffff0000u) : (s2[j >> 1] << 16));
        const float z = __uint_as_float((j & 1) ? (z2[j >> 1] & 0xffff0000u) : (z2[j >> 1] << 16));
        S[j] = fmul(s, 16777216.0f);
        C[j] = fmul(-z, s);
    }
}
struct PCtx { unsigned vq, vx, m0a, wa0, wa1; unsigned xo[4]; __amdgpu_buffer_rsrc_t srdQ, srdX; };
template <bool ISSUE>
DI void pstep(u32x4 (&q)[8], u32x4 (&qn)[8], const float (&S)[4], const float (&C)[4], const PCtx& c, int bufoff, int ns) {
    u32x4 o[4];
    const unsigned sq = (unsigned)ns * (BK * QROW), sx = (unsigned)ns * (BK * 2), mb = c.m0a + (unsigned)(ns & 3) * A_STAGE;
    if constexpr (ISSUE) {
#pragma unroll
        for (int i = 0; i < QFIRST; ++i) ld_q(qn[i], c.vq, c.srdQ, sq + (unsigned)i * QROW);
    }
    wait_q<ISSUE ? 8 + QFIRST : 8>(q);
#pragma unroll
    for (int kk = 0; kk < 4; ++kk) {
        dequant_pair(o, kk, q[2 * kk], q[2 * kk + 1], S, C);
        if (kk == 3) {
#pragma unroll
            for (int j = 0; j < 4; ++j) *(LAS u32x4*)(uintptr_t)(((j >> 1) ? c.wa1 : c.wa0) + (unsigned)(j * 128 + bufoff)) = o[j];
        }
        if constexpr (ISSUE) {
            if (QFIRST == 8) {
                dma16(mb + c.xo[kk] * 4096u, c.vx, c.srdX, sx + c.xo[kk] * (32u * XROW));
                dma16(mb + c.xo[kk] * 4096u + 4096u, c.vx, c.srdX, sx + c.xo[kk] * (32u * XROW) + 32u * XROW);
            } else {
                constexpr int seq[12] = {4, 5, 6, 7, 8, 9, 10, 11, 12, 13, 14, 15};
#pragma unroll
                for (int u = 0; u < 3; ++u) {
                    const int e = seq[kk * 3 + u];
                    if (e < 8) ld_q(qn[e], c.vq, c.srdQ, sq + (unsigned)e * QROW);
                    else { const unsigned pj = c.xo[(e - 8) >> 1] + ((e - 8) & 1); dma16(mb + pj * 4096u, c.vx, c.srdX, sx + pj * (32u * XROW)); }
                }
            }
        }
    }
    if constexpr (ISSUE) WAIT_V(16); else WAIT_V(0);
}

struct Frag { bf16x8 x[4]; bf16x8 w[8]; };
DI void read_frags(Frag& f, unsigned xa, unsigned w0, unsigned w1, int bufoff) {
    f.x[0] = *(LAS const bf16x8*)(uintptr_t)(xa);
#pragma unroll
    for (int t = 0; t < 8; ++t)
        f.w[t] = *(LAS const bf16x8*)(uintptr_t)(((t & 1) ? w1 : w0) + (unsigned)(t * 2048 + bufoff));
#pragma unroll
    for (int m = 1; m < 4; ++m)
        f.x[m] = *(LAS const bf16x8*)(uintptr_t)(xa + (unsigned)(m * 2048));
}
DI void mma(f32x4 (&acc)[4][8], const Frag& f) {
#pragma unroll
    for (int m = 0; m < 4; ++m)
#pragma unroll
        for (int t = 0; t < 8; ++t)
            acc[m][t] = __builtin_amdgcn_mfma_f32_16x16x32_bf16(f.w[t], f.x[m], acc[m][t], 0, 0, 0);
}
DI void interleave() {
#pragma unroll
    for (int i = 0; i < 12; ++i) {
        __builtin_amdgcn_sched_group_barrier(0x008, 2, 0);
        __builtin_amdgcn_sched_group_barrier(0x100, 1, 0);
    }
    __builtin_amdgcn_sched_group_barrier(0x008, 8, 0);
}

__global__ __launch_bounds__(512, 2)
void w4a16_kernel(const unsigned short* __restrict__ x, const int* __restrict__ qw, const unsigned short* __restrict__ sc,
                  const unsigned short* __restrict__ zr, unsigned short* __restrict__ out, char* __restrict__ ws) {
    __shared__ __attribute__((aligned(1024))) char smem[LDS_TOTAL];
    const unsigned lbase = (unsigned)(uintptr_t)(LAS char*)smem;
    const int lane = threadIdx.x & 63;
    const int wid = __builtin_amdgcn_readfirstlane(threadIdx.x >> 6);
    const int bid = blockIdx.x, xcd = bid & 7, idx = bid >> 3;
    const int tn0 = (xcd >> 1) * 7 + (xcd & 1) * 3;
    const __amdgpu_buffer_rsrc_t srdQ = __builtin_amdgcn_make_buffer_rsrc((void*)qw, (short)0, K_ * N_ * 4, 0x00020000);
    STAMP_DECL;
    STAMP(0);
    if (idx >= 28) {
        if (HELP) {
            const unsigned v = (unsigned)((lane >> 4) * QROW + tn0 * 512 + (lane & 15) * 128);
            for (int row = HSTART * BK + ((idx - 28) * 8 + wid) * 4; row < K_; row += 128)
                dma4(lbase, v, srdQ, (unsigned)row * QROW);
            WAIT_V(0);
        }
        STAMP(21);
        STAMP_OUT();
        return;
    }
    int tm, tn;
    if (idx < 24) { tm = idx & 7; tn = tn0 + (xcd & 1) + (idx >> 3); }
    else { tm = (xcd & 1) * 4 + (idx - 24); tn = (xcd >> 1) * 7 + 3; }
    const int m0 = tm * BM, n0 = tn * BN;

    if (wid >= 4) {
        const int pw = wid - 4;
        PCtx c;
        c.srdQ = srdQ;
        c.srdX = __builtin_amdgcn_make_buffer_rsrc((void*)x, (short)0, M_ * K_ * 2, 0x00020000);
        const __amdgpu_buffer_rsrc_t srdS = __builtin_amdgcn_make_buffer_rsrc((void*)sc, (short)0, 8 * N_ * 2, 0x00020000);
        const __amdgpu_buffer_rsrc_t srdZ = __builtin_amdgcn_make_buffer_rsrc((void*)zr, (short)0, 8 * N_ * 2, 0x00020000);
        const int kb = 2 * pw + (lane >> 5), nb = lane & 31;
        c.vq = (unsigned)(kb * 8 * QROW + (n0 + 4 * nb) * 4);
        const unsigned vs = (unsigned)((n0 + 4 * nb) * 2);
        const int rin = pw * 8 + (lane >> 3);
        c.vx = (unsigned)((m0 + rin) * XROW + (((lane & 7) ^ ((rin >> 1) & 7)) * 16));
        c.m0a = lbase + LDS_A + pw * 1024;
#pragma unroll
        for (int k = 0; k < 4; ++k) c.xo[k] = (unsigned)(((k + (XROT ? (idx >> 3) : 0)) & 3) * 2);
        u32x4 q0[8], q1[8]; u32x2 s2, z2;
        ld8(s2, vs, srdS, 0u); ld8(z2, vs, srdZ, 0u);
#pragma unroll
        for (int i = 0; i < 8; ++i) ld_q(q0[i], c.vq, c.srdQ, (unsigned)i * QROW);
#pragma unroll
        for (int j = 0; j < 8; ++j) dma16(c.m0a + (unsigned)(j * 4096), c.vx, c.srdX, (unsigned)(j * 32 * XROW));
        {
            const int l = (tn * 4 + pw) * 37 + lane;
            dma4(lbase + LDS_B + B_STAGE, (PFX && lane < 37 && l < 4096) ? (unsigned)((m0 + (l & 255)) * XROW + (l >> 8) * 128) : 0x80000000u, c.srdX, 0u);
        }
        const int rb = 32 * (nb >> 3) + 16 * (nb & 1) + 4 * ((nb >> 1) & 3);
        const int f = ((rb >> 1) & 7) ^ ((rb >> 4) & 1);
        c.wa0 = lbase + (unsigned)(LDS_B + rb * 128 + ((kb ^ f) * 16));
        c.wa1 = c.wa0 ^ 16u;
        STAMP(1);
        float S[4], C[4];
        asm volatile("s_waitcnt vmcnt(17)" : "+v"(s2), "+v"(z2) :: "memory");
        make_scales(S, C, s2, z2);
        for (int b = 0; b < NIT - 2; b += 2) {
            ld8(s2, vs, srdS, (unsigned)((b / 2 + 1) * N_ * 2)); ld8(z2, vs, srdZ, (unsigned)((b / 2 + 1) * N_ * 2));
            pstep<true>(q0, q1, S, C, c, 0, b + 1);
            WAIT_L0();
            BAR();
            LSTAMP(2 + b);
            pstep<true>(q1, q0, S, C, c, B_STAGE, b + 2);
            asm volatile("" : "+v"(s2), "+v"(z2) :: "memory");
            make_scales(S, C, s2, z2);
            WAIT_L0();
            BAR();
            LSTAMP(3 + b);
        }
        pstep<true>(q0, q1, S, C, c, 0, NIT - 1);
        WAIT_L0();
        BAR();
        LSTAMP(16);
        pstep<false>(q1, q0, S, C, c, B_STAGE, 0);
        WAIT_L0();
        BAR();
        STAMP(17);
        BAR();
    } else {
        const int w = wid;
        const int fr = lane & 15, fq = lane >> 4;
        const unsigned sw = (unsigned)((fq ^ (fr >> 1)) * 16);
        unsigned xa0 = lbase + (unsigned)(LDS_A + (w * 64 + fr) * 128) + sw;
        unsigned w00 = lbase + (unsigned)(LDS_B + fr * 128) + sw;
        unsigned w01 = w00 ^ 16u, w10 = w00 ^ 64u, w11 = w00 ^ 80u;
        asm volatile("" : "+v"(w00), "+v"(w01), "+v"(w10), "+v"(w11));
        f32x4 acc[4][8];
#pragma unroll
        for (int m = 0; m < 4; ++m)
#pragma unroll
            for (int t = 0; t < 8; ++t) { acc[m][t] = (f32x4){0.f, 0.f, 0.f, 0.f}; asm volatile("" : "+v"(acc[m][t])); }
        Frag f0, f1;
        asm volatile("" :: "s"(out));
        BAR();
        STAMP(2);
        read_frags(f0, xa0, w00, w01, 0);
        for (int it = 0; it < NIT; it += 2) {
            unsigned xe = xa0 + (unsigned)((it & 2) * A_STAGE);
            unsigned xn = xa0 + (unsigned)(((it + 2) & 2) * A_STAGE);
            asm volatile("" : "+v"(xe), "+v"(xn));
            SB0();
            read_frags(f1, xe ^ 64u, w10, w11, 0);
            mma(acc, f0);
            interleave();
            SB0();
            WAIT_L0();
            BAR();
            LSTAMP(3 + it);
            SB0();
            read_frags(f0, xe + A_STAGE, w00, w01, B_STAGE);
            mma(acc, f1);
            interleave();
            SB0();
            read_frags(f1, (xe ^ 64u) + A_STAGE, w10, w11, B_STAGE);
            mma(acc, f0);
            interleave();
            SB0();
            WAIT_L0();
            BAR();
            LSTAMP(4 + it);
            SB0();
            read_frags(f0, xn, w00, w01, 0);
            mma(acc, f1);
            interleave();
            SB0();
        }
        STAMP(19);
        const unsigned eb = lbase + (unsigned)(A_STAGE + w * (64 * OROW));
        const unsigned ew = eb + (unsigned)(fr * OROW + fq * 16);
        const unsigned er = eb + (unsigned)(fq * OROW + fr * 16);
        const __amdgpu_buffer_rsrc_t srdO = __builtin_amdgcn_make_buffer_rsrc((void*)out, (short)0, M_ * N_ * 2, 0x00020000);
        const int ovo = fq * (N_ * 2) + fr * 16;
        const int oso = ((m0 + w * 64) * N_ + n0) * 2;
#pragma unroll
        for (int m = 0; m < 4; ++m) {
#pragma unroll
            for (int pp = 0; pp < 4; ++pp) {
                u32x4 v;
                v[0] = cvt_pk_bf16(acc[m][2 * pp][0], acc[m][2 * pp][1]);
                v[1] = cvt_pk_bf16(acc[m][2 * pp][2], acc[m][2 * pp][3]);
                v[2] = cvt_pk_bf16(acc[m][2 * pp + 1][0], acc[m][2 * pp + 1][1]);
                v[3] = cvt_pk_bf16(acc[m][2 * pp + 1][2], acc[m][2 * pp + 1][3]);
                *(LAS u32x4*)(uintptr_t)(ew + (unsigned)(m * 16 * OROW + pp * 64)) = v;
            }
#pragma unroll
            for (int i = 0; i < 4; ++i) {
                const u32x4 v = *(LAS const u32x4*)(uintptr_t)(er + (unsigned)((m * 16 + i * 4) * OROW));
                __builtin_amdgcn_raw_buffer_store_b128(v, srdO, ovo, oso + (m * 16 + i * 4) * (N_ * 2), STAUX);
            }
        }
        STAMP(20);
        STAMP(21);
    }
    STAMP_OUT();
}
}

extern "C" void kernel_launch(void* const* d_in, const int* in_sizes, int n_in,
                              void* d_out, int out_size, void* d_ws, size_t ws_size,
                              hipStream_t stream) {
    w4a16_kernel<<<256, 512, 0, stream>>>((const unsigned short*)d_in[0], (const int*)d_in[1],
        (const unsigned short*)d_in[2], (const unsigned short*)d_in[3], (unsigned short*)d_out, (char*)d_ws);
}
```

```cpp
#include <hip/hip_runtime.h>
#include <hip/hip_bf16.h>

typedef short bf16x8 __attribute__((ext_vector_type(8)));
typedef float f32x4 __attribute__((ext_vector_type(4)));
typedef unsigned u32x4 __attribute__((ext_vector_type(4)));
typedef unsigned u32x2 __attribute__((ext_vector_type(2)));
#define LAS __attribute__((address_space(3)))
#define DI __device__ __forceinline__

#define QFIRST 4
#define COOP 1
#define HELPX 1
#define HELP 1
#define HSTART COOP
#define PFX 0
#define XROT 1
#define STAUX 2

namespace {
constexpr int M_ = 2048, N_ = 3584, K_ = 1024;
constexpr int BM = 256, BN = 128, BK = 64, NIT = K_ / BK;
constexpr int A_STAGE = BM * BK * 2;
constexpr int B_STAGE = BN * BK * 2;
constexpr int LDS_A = 0, LDS_B = 4 * A_STAGE;
constexpr int LDS_TOTAL = LDS_B + 2 * B_STAGE;
constexpr int QROW = N_ * 4;
constexpr int XROW = K_ * 2;
constexpr int OROW = 272;

#define STAMP(i)
#define STAMP_DECL
#define STAMP_OUT()
#define LSTAMP(i) STAMP(i)
#define WAIT_V(n) asm volatile("s_waitcnt vmcnt(%0)" ::"n"(n) : "memory")
#define WAIT_L0() asm volatile("s_waitcnt lgkmcnt(0)" ::: "memory")
#define BAR() do { asm volatile("" ::: "memory"); __builtin_amdgcn_s_barrier(); asm volatile("" ::: "memory"); } while (0)
#define SB0() __builtin_amdgcn_sched_barrier(0)

DI unsigned cvt_pk_bf16(float lo, float hi) { unsigned r; asm("v_cvt_pk_bf16_f32 %0, %1, %2" : "=v"(r) : "v"(lo), "v"(hi)); return r; }
DI float fmul(float a, float b) { float r; asm volatile("v_mul_f32 %0, %1, %2" : "=v"(r) : "v"(a), "v"(b)); return r; }
DI void ld_q(u32x4& q, unsigned vq, __amdgpu_buffer_rsrc_t srd, unsigned so) {
    asm volatile("buffer_load_dwordx4 %0, %1, %2, %3 offen" : "=v"(q) : "v"(vq), "s"(srd), "s"(so) : "memory");
}
DI void ld8(u32x2& q, unsigned v, __amdgpu_buffer_rsrc_t srd, unsigned so) {
    asm volatile("buffer_load_dwordx2 %0, %1, %2, %3 offen" : "=v"(q) : "v"(v), "s"(srd), "s"(so) : "memory");
}
DI void dma16(unsigned m0v, unsigned v, __amdgpu_buffer_rsrc_t srd, unsigned so) {
    asm volatile("s_mov_b32 m0, %0\n\ts_nop 0\n\tbuffer_load_dwordx4 %1, %2, %3 offen lds" :: "s"(m0v), "v"(v), "s"(srd), "s"(so) : "memory");
}
DI void dma4(unsigned m0v, unsigned v, __amdgpu_buffer_rsrc_t srd, unsigned so) {
    asm volatile("s_mov_b32 m0, %0\n\ts_nop 0\n\tbuffer_load_dword %1, %2, %3 offen lds" :: "s"(m0v), "v"(v), "s"(srd), "s"(so) : "memory");
}
template <int W> DI void wait_q(u32x4 (&q)[8]) {
    asm volatile("s_waitcnt vmcnt(%8)"
                 : "+v"(q[0]), "+v"(q[1]), "+v"(q[2]), "+v"(q[3]), "+v"(q[4]), "+v"(q[5]), "+v"(q[6]), "+v"(q[7]) : "n"(W) : "memory");
}
DI void dequant_pair(u32x4 (&o)[4], int kk, const u32x4& qa, const u32x4& qb, const float (&S)[4], const float (&C)[4]) {
    float t0, t1, t2, t3, t4, t5, t6, t7;
    asm volatile(
        "v_fma_mix_f32 %4, %12, %20, %24 op_sel_hi:[1,0,0]\n\t"
        "v_fma_mix_f32 %5, %16, %20, %24 op_sel_hi:[1,0,0]\n\t"
        "v_fma_mix_f32 %6, %13, %21, %25 op_sel_hi:[1,0,0]\n\t"
        "v_fma_mix_f32 %7, %17, %21, %25 op_sel_hi:[1,0,0]\n\t"
        "v_fma_mix_f32 %8, %14, %22, %26 op_sel_hi:[1,0,0]\n\t"
        "v_fma_mix_f32 %9, %18, %22, %26 op_sel_hi:[1,0,0]\n\t"
        "v_fma_mix_f32 %10, %15, %23, %27 op_sel_hi:[1,0,0]\n\t"
        "v_fma_mix_f32 %11, %19, %23, %27 op_sel_hi:[1,0,0]\n\t"
        "v_cvt_pk_bf16_f32 %0, %4, %5\n\t"
        "v_cvt_pk_bf16_f32 %1, %6, %7\n\t"
        "v_cvt_pk_bf16_f32 %2, %8, %9\n\t"
        "v_cvt_pk_bf16_f32 %3, %10, %11"
        : "=&v"(o[0][kk]), "=&v"(o[1][kk]), "=&v"(o[2][kk]), "=&v"(o[3][kk]),
          "=&v"(t0), "=&v"(t1), "=&v"(t2), "=&v"(t3), "=&v"(t4), "=&v"(t5), "=&v"(t6), "=&v"(t7)
        : "v"(qa[0]), "v"(qa[1]), "v"(qa[2]), "v"(qa[3]), "v"(qb[0]), "v"(qb[1]), "v"(qb[2]), "v"(qb[3]),
          "v"(S[0]), "v"(S[1]), "v"(S[2]), "v"(S[3]), "v"(C[0]), "v"(C[1]), "v"(C[2]), "v"(C[3]));
}
DI void make_scales(float (&S)[4], float (&C)[4], const u32x2& s2, const u32x2& z2) {
#pragma unroll
    for (int j = 0; j < 4; ++j) {
        const float s = __uint_as_float((j & 1) ? (s2[j >> 1] & 0xffff0000u) : (s2[j >> 1] << 16));
        const float z = __uint_as_float((j & 1) ? (z2[j >> 1] & 0xffff0000u) : (z2[j >> 1] << 16));
        S[j] = fmul(s, 16777216.0f);
        C[j] = fmul(-z, s);
    }
}
struct PCtx { unsigned vq, vx, m0a, wa0, wa1; unsigned xo[4]; __amdgpu_buffer_rsrc_t srdQ, srdX; };
template <bool ISSUE>
DI void pstep(u32x4 (&q)[8], u32x4 (&qn)[8], const float (&S)[4], const float (&C)[4], const PCtx& c, int bufoff, int ns) {
    u32x4 o[4];
    const unsigned sq = (unsigned)ns * (BK * QROW), sx = (unsigned)ns * (BK * 2), mb = c.m0a + (unsigned)(ns & 3) * A_STAGE;
    if constexpr (ISSUE) {
#pragma unroll
        for (int i = 0; i < QFIRST; ++i) ld_q(qn[i], c.vq, c.srdQ, sq + (unsigned)i * QROW);
    }
    wait_q<ISSUE ? 8 + QFIRST : 8>(q);
#pragma unroll
    for (int kk = 0; kk < 4; ++kk) {
        dequant_pair(o, kk, q[2 * kk], q[2 * kk + 1], S, C);
        if (kk == 3) {
#pragma unroll
            for (int j = 0; j < 4; ++j) *(LAS u32x4*)(uintptr_t)(((j >> 1) ? c.wa1 : c.wa0) + (unsigned)(j * 128 + bufoff)) = o[j];
        }
        if constexpr (ISSUE) {
            if (QFIRST == 8) {
                dma16(mb + c.xo[kk] * 4096u, c.vx, c.srdX, sx + c.xo[kk] * (32u * XROW));
                dma16(mb + c.xo[kk] * 4096u + 4096u, c.vx, c.srdX, sx + c.xo[kk] * (32u * XROW) + 32u * XROW);
            } else {
                constexpr int seq[12] = {4, 5, 6, 7, 8, 9, 10, 11, 12, 13, 14, 15};
#pragma unroll
                for (int u = 0; u < 3; ++u) {
                    const int e = seq[kk * 3 + u];
                    if (e < 8) ld_q(qn[e], c.vq, c.srdQ, sq + (unsigned)e * QROW);
                    else { const unsigned pj = c.xo[(e - 8) >> 1] + ((e - 8) & 1); dma16(mb + pj * 4096u, c.vx, c.srdX, sx + pj * (32u * XROW)); }
                }
            }
        }
    }
    if constexpr (ISSUE) WAIT_V(16); else WAIT_V(0);
}

struct Frag { bf16x8 x[4]; bf16x8 w[8]; };
DI void read_frags(Frag& f, unsigned xa, unsigned w0, unsigned w1, int bufoff) {
    f.x[0] = *(LAS const bf16x8*)(uintptr_t)(xa);
#pragma unroll
    for (int t = 0; t < 8; ++t)
        f.w[t] = *(LAS const bf16x8*)(uintptr_t)(((t & 1) ? w1 : w0) + (unsigned)(t * 2048 + bufoff));
#pragma unroll
    for (int m = 1; m < 4; ++m)
        f.x[m] = *(LAS const bf16x8*)(uintptr_t)(xa + (unsigned)(m * 2048));
}
DI void mma(f32x4 (&acc)[4][8], const Frag& f) {
#pragma unroll
    for (int m = 0; m < 4; ++m)
#pragma unroll
        for (int t = 0; t < 8; ++t)
            acc[m][t] = __builtin_amdgcn_mfma_f32_16x16x32_bf16(f.w[t], f.x[m], acc[m][t], 0, 0, 0);
}
DI void interleave() {
#pragma unroll
    for (int i = 0; i < 12; ++i) {
        __builtin_amdgcn_sched_group_barrier(0x008, 2, 0);
        __builtin_amdgcn_sched_group_barrier(0x100, 1, 0);
    }
    __builtin_amdgcn_sched_group_barrier(0x008, 8, 0);
}

__global__ __launch_bounds__(512, 2)
void w4a16_kernel(const unsigned short* __restrict__ x, const int* __restrict__ qw, const unsigned short* __restrict__ sc,
                  const unsigned short* __restrict__ zr, unsigned short* __restrict__ out, char* __restrict__ ws) {
    __shared__ __attribute__((aligned(1024))) char smem[LDS_TOTAL];
    const unsigned lbase = (unsigned)(uintptr_t)(LAS char*)smem;
    const int lane = threadIdx.x & 63;
    const int wid = __builtin_amdgcn_readfirstlane(threadIdx.x >> 6);
    const int bid = blockIdx.x, xcd = bid & 7, idx = bid >> 3;
    const int tn0 = (xcd >> 1) * 7 + (xcd & 1) * 3;
    const __amdgpu_buffer_rsrc_t srdQ = __builtin_amdgcn_make_buffer_rsrc((void*)qw, (short)0, K_ * N_ * 4, 0x00020000);
    STAMP_DECL;
    STAMP(0);
    if (idx >= 28) {
        if (HELP && wid == 0) {
            const __amdgpu_buffer_rsrc_t srdXh = __builtin_amdgcn_make_buffer_rsrc((void*)x, (short)0, M_ * K_ * 2, 0x00020000);
            const int h = idx - 28;
            const unsigned v = (unsigned)((h * 16 + (lane >> 4)) * QROW + tn0 * 512 + (lane & 15) * 128);
            const unsigned vxh = (unsigned)((xcd * 256 + h * 64 + lane) * XROW);
            for (int s = HSTART; s < NIT; ++s) {
                if (HELPX) dma4(lbase, vxh, srdXh, (unsigned)(s * 128));
#pragma unroll
                for (int u = 0; u < 4; ++u) dma4(lbase, v, srdQ, (unsigned)(s * BK + u * 4) * QROW);
            }
            WAIT_V(0);
        }
        STAMP(21);
        STAMP_OUT();
        return;
    }
    int tm, tn;
    if (idx < 24) { tm = idx & 7; tn = tn0 + (xcd & 1) + (idx >> 3); }
    else { tm = (xcd & 1) * 4 + (idx - 24); tn = (xcd >> 1) * 7 + 3; }
    const int m0 = tm * BM, n0 = tn * BN;

    if (wid >= 4) {
        const int pw = wid - 4;
        PCtx c;
        c.srdQ = srdQ;
        c.srdX = __builtin_amdgcn_make_buffer_rsrc((void*)x, (short)0, M_ * K_ * 2, 0x00020000);
        const __amdgpu_buffer_rsrc_t srdS = __builtin_amdgcn_make_buffer_rsrc((void*)sc, (short)0, 8 * N_ * 2, 0x00020000);
        const __amdgpu_buffer_rsrc_t srdZ = __builtin_amdgcn_make_buffer_rsrc((void*)zr, (short)0, 8 * N_ * 2, 0x00020000);
        const int kb = 2 * pw + (lane >> 5), nb = lane & 31;
        c.vq = (unsigned)(kb * 8 * QROW + (n0 + 4 * nb) * 4);
        const unsigned vs = (unsigned)((n0 + 4 * nb) * 2);
        const int rin = pw * 8 + (lane >> 3);
        c.vx = (unsigned)((m0 + rin) * XROW + (((lane & 7) ^ ((rin >> 1) & 7)) * 16));
        c.m0a = lbase + LDS_A + pw * 1024;
#pragma unroll
        for (int k = 0; k < 4; ++k) c.xo[k] = (unsigned)(((k + (XROT ? (idx >> 3) : 0)) & 3) * 2);
        u32x4 q0[8], q1[8]; u32x2 s2, z2;
        ld8(s2, vs, srdS, 0u); ld8(z2, vs, srdZ, 0u);
#pragma unroll
        for (int i = 0; i < 8; ++i) ld_q(q0[i], c.vq, c.srdQ, (unsigned)i * QROW);
#pragma unroll
        for (int j = 0; j < 8; ++j) dma16(c.m0a + (unsigned)(j * 4096), c.vx, c.srdX, (unsigned)(j * 32 * XROW));
        {
            const int l = (tn * 4 + pw) * 37 + lane;
            dma4(lbase + LDS_B + B_STAGE, (PFX && lane < 37 && l < 4096) ? (unsigned)((m0 + (l & 255)) * XROW + (l >> 8) * 128) : 0x80000000u, c.srdX, 0u);
        }
        const int rb = 32 * (nb >> 3) + 16 * (nb & 1) + 4 * ((nb >> 1) & 3);
        const int f = ((rb >> 1) & 7) ^ ((rb >> 4) & 1);
        c.wa0 = lbase + (unsigned)(LDS_B + rb * 128 + ((kb ^ f) * 16));
        c.wa1 = c.wa0 ^ 16u;
        STAMP(1);
        float S[4], C[4];
        asm volatile("s_waitcnt vmcnt(17)" : "+v"(s2), "+v"(z2) :: "memory");
        make_scales(S, C, s2, z2);
        for (int b = 0; b < NIT - 2; b += 2) {
            ld8(s2, vs, srdS, (unsigned)((b / 2 + 1) * N_ * 2)); ld8(z2, vs, srdZ, (unsigned)((b / 2 + 1) * N_ * 2));
            pstep<true>(q0, q1, S, C, c, 0, b + 1);
            WAIT_L0();
            BAR();
            LSTAMP(2 + b);
            pstep<true>(q1, q0, S, C, c, B_STAGE, b + 2);
            asm volatile("" : "+v"(s2), "+v"(z2) :: "memory");
            make_scales(S, C, s2, z2);
            WAIT_L0();
            BAR();
            LSTAMP(3 + b);
        }
        pstep<true>(q0, q1, S, C, c, 0, NIT - 1);
        WAIT_L0();
        BAR();
        LSTAMP(16);
        pstep<false>(q1, q0, S, C, c, B_STAGE, 0);
        WAIT_L0();
        BAR();
        STAMP(17);
        BAR();
    } else {
        const int w = wid;
        if (COOP) {
            constexpr int LPW = (COOP * 3072 + 111) / 112;
            const int l = (idx * 4 + w) * LPW + lane;
            if (lane < LPW && l < COOP * 3072) {
                const int lx = l - COOP * 1024;
                const char* p = lx < 0 ? (const char*)qw + (size_t)(l >> 4) * QROW + tn0 * 512 + (l & 15) * 128
                                       : (const char*)x + (size_t)(lx / COOP) * XROW + (lx % COOP) * 128;
                const unsigned v = *(const volatile unsigned*)p;
                asm volatile("" :: "v"(v));
            }
        }
        const int fr = lane & 15, fq = lane >> 4;
        const unsigned sw = (unsigned)((fq ^ (fr >> 1)) * 16);
        unsigned xa0 = lbase + (unsigned)(LDS_A + (w * 64 + fr) * 128) + sw;
        unsigned w00 = lbase + (unsigned)(LDS_B + fr * 128) + sw;
        unsigned w01 = w00 ^ 16u, w10 = w00 ^ 64u, w11 = w00 ^ 80u;
        asm volatile("" : "+v"(w00), "+v"(w01), "+v"(w10), "+v"(w11));
        f32x4 acc[4][8];
#pragma unroll
        for (int m = 0; m < 4; ++m)
#pragma unroll
            for (int t = 0; t < 8; ++t) { acc[m][t] = (f32x4){0.f, 0.f, 0.f, 0.f}; asm volatile("" : "+v"(acc[m][t])); }
        Frag f0, f1;
        asm volatile("" :: "s"(out));
        BAR();
        STAMP(2);
        read_frags(f0, xa0, w00, w01, 0);
        for (int it = 0; it < NIT; it += 2) {
            unsigned xe = xa0 + (unsigned)((it & 2) * A_STAGE);
            unsigned xn = xa0 + (unsigned)(((it + 2) & 2) * A_STAGE);
            asm volatile("" : "+v"(xe), "+v"(xn));
            SB0();
            read_frags(f1, xe ^ 64u, w10, w11, 0);
            mma(acc, f0);
            interleave();
            SB0();
            WAIT_L0();
            BAR();
            LSTAMP(3 + it);
            SB0();
            read_frags(f0, xe + A_STAGE, w00, w01, B_STAGE);
            mma(acc, f1);
            interleave();
            SB0();
            read_frags(f1, (xe ^ 64u) + A_STAGE, w10, w11, B_STAGE);
            mma(acc, f0);
            interleave();
            SB0();
            WAIT_L0();
            BAR();
            LSTAMP(4 + it);
            SB0();
            read_frags(f0, xn, w00, w01, 0);
            mma(acc, f1);
            interleave();
            SB0();
        }
        STAMP(19);
        const unsigned eb = lbase + (unsigned)(A_STAGE + w * (64 * OROW));
        const unsigned ew = eb + (unsigned)(fr * OROW + fq * 16);
        const unsigned er = eb + (unsigned)(fq * OROW + fr * 16);
        const __amdgpu_buffer_rsrc_t srdO = __builtin_amdgcn_make_buffer_rsrc((void*)out, (short)0, M_ * N_ * 2, 0x00020000);
        const int ovo = fq * (N_ * 2) + fr * 16;
        const int oso = ((m0 + w * 64) * N_ + n0) * 2;
#pragma unroll
        for (int m = 0; m < 4; ++m) {
#pragma unroll
            for (int pp = 0; pp < 4; ++pp) {
                u32x4 v;
                v[0] = cvt_pk_bf16(acc[m][2 * pp][0], acc[m][2 * pp][1]);
                v[1] = cvt_pk_bf16(acc[m][2 * pp][2], acc[m][2 * pp][3]);
                v[2] = cvt_pk_bf16(acc[m][2 * pp + 1][0], acc[m][2 * pp + 1][1]);
                v[3] = cvt_pk_bf16(acc[m][2 * pp + 1][2], acc[m][2 * pp + 1][3]);
                *(LAS u32x4*)(uintptr_t)(ew + (unsigned)(m * 16 * OROW + pp * 64)) = v;
            }
#pragma unroll
            for (int i = 0; i < 4; ++i) {
                const u32x4 v = *(LAS const u32x4*)(uintptr_t)(er + (unsigned)((m * 16 + i * 4) * OROW));
                __builtin_amdgcn_raw_buffer_store_b128(v, srdO, ovo, oso + (m * 16 + i * 4) * (N_ * 2), STAUX);
            }
        }
        STAMP(20);
        STAMP(21);
    }
    STAMP_OUT();
}
}

extern "C" void kernel_launch(void* const* d_in, const int* in_sizes, int n_in,
                              void* d_out, int out_size, void* d_ws, size_t ws_size,
                              hipStream_t stream) {
    w4a16_kernel<<<256, 512, 0, stream>>>((const unsigned short*)d_in[0], (const int*)d_in[1],
        (const unsigned short*)d_in[2], (const unsigned short*)d_in[3], (unsigned short*)d_out, (char*)d_ws);
}
```

```cpp
#include <hip/hip_runtime.h>
#include <hip/hip_bf16.h>
#include <type_traits>

typedef short bf16x8 __attribute__((ext_vector_type(8)));
typedef float f32x4 __attribute__((ext_vector_type(4)));
typedef unsigned u32x4 __attribute__((ext_vector_type(4)));
typedef unsigned u32x2 __attribute__((ext_vector_type(2)));
#define LAS __attribute__((address_space(3)))
#define DI __device__ __forceinline__

#define QFIRST 4
#define CQ 1
#define CX0 1
#define HELPX 1
#define HELPS 1
#define SEQ 2
#define HELP 1
#define HSTART CQ
#define PFX 0
#define XROT 1
#define STAUX 2

namespace {
constexpr int M_ = 2048, N_ = 3584, K_ = 1024;
constexpr int BM = 256, BN = 128, BK = 64, NIT = K_ / BK;
constexpr int A_STAGE = BM * BK * 2;
constexpr int B_STAGE = BN * BK * 2;
constexpr int LDS_A = 0, LDS_B = 4 * A_STAGE;
constexpr int LDS_TOTAL = LDS_B + 2 * B_STAGE;
constexpr int QROW = N_ * 4;
constexpr int XROW = K_ * 2;
constexpr int OROW = 272;

#define STAMP(i)
#define STAMP_DECL
#define STAMP_OUT()
#define LSTAMP(i) STAMP(i)
#define PSTAMP(i) LSTAMP(i)
#define WAIT_V(n) asm volatile("s_waitcnt vmcnt(%0)" ::"n"(n) : "memory")
#define WAIT_L0() asm volatile("s_waitcnt lgkmcnt(0)" ::: "memory")
#define BAR() do { asm volatile("" ::: "memory"); __builtin_amdgcn_s_barrier(); asm volatile("" ::: "memory"); } while (0)
#define SB0() __builtin_amdgcn_sched_barrier(0)

DI unsigned cvt_pk_bf16(float lo, float hi) { unsigned r; asm("v_cvt_pk_bf16_f32 %0, %1, %2" : "=v"(r) : "v"(lo), "v"(hi)); return r; }
DI float fmul(float a, float b) { float r; asm volatile("v_mul_f32 %0, %1, %2" : "=v"(r) : "v"(a), "v"(b)); return r; }
DI void ld_q(u32x4& q, unsigned vq, __amdgpu_buffer_rsrc_t srd, unsigned so) {
    asm volatile("buffer_load_dwordx4 %0, %1, %2, %3 offen" : "=v"(q) : "v"(vq), "s"(srd), "s"(so) : "memory");
}
DI void ld8(u32x2& q, unsigned v, __amdgpu_buffer_rsrc_t srd, unsigned so) {
    asm volatile("buffer_load_dwordx2 %0, %1, %2, %3 offen" : "=v"(q) : "v"(v), "s"(srd), "s"(so) : "memory");
}
DI void dma16(unsigned m0v, unsigned v, __amdgpu_buffer_rsrc_t srd, unsigned so) {
    asm volatile("s_mov_b32 m0, %0\n\ts_nop 0\n\tbuffer_load_dwordx4 %1, %2, %3 offen lds" :: "s"(m0v), "v"(v), "s"(srd), "s"(so) : "memory");
}
DI void set_m0(unsigned m0v) { asm volatile("s_mov_b32 m0, %0" :: "s"(m0v) : "memory"); }
template <int OFF> DI void dma16o(unsigned v, __amdgpu_buffer_rsrc_t srd, unsigned so) {
    asm volatile("buffer_load_dwordx4 %0, %1, %2 offen offset:%3 lds" :: "v"(v), "s"(srd), "s"(so), "n"(OFF) : "memory");
}
DI void dma4(unsigned m0v, unsigned v, __amdgpu_buffer_rsrc_t srd, unsigned so) {
    asm volatile("s_mov_b32 m0, %0\n\ts_nop 0\n\tbuffer_load_dword %1, %2, %3 offen lds" :: "s"(m0v), "v"(v), "s"(srd), "s"(so) : "memory");
}
template <int W> DI void wait_q(u32x4 (&q)[8]) {
    asm volatile("s_waitcnt vmcnt(%8)"
                 : "+v"(q[0]), "+v"(q[1]), "+v"(q[2]), "+v"(q[3]), "+v"(q[4]), "+v"(q[5]), "+v"(q[6]), "+v"(q[7]) : "n"(W) : "memory");
}
DI void dequant_pair(u32x4 (&o)[4], int kk, const u32x4& qa, const u32x4& qb, const float (&S)[4], const float (&C)[4]) {
    float t0, t1, t2, t3, t4, t5, t6, t7;
    asm volatile(
        "v_fma_mix_f32 %4, %12, %20, %24 op_sel_hi:[1,0,0]\n\t"
        "v_fma_mix_f32 %5, %16, %20, %24 op_sel_hi:[1,0,0]\n\t"
        "v_fma_mix_f32 %6, %13, %21, %25 op_sel_hi:[1,0,0]\n\t"
        "v_fma_mix_f32 %7, %17, %21, %25 op_sel_hi:[1,0,0]\n\t"
        "v_fma_mix_f32 %8, %14, %22, %26 op_sel_hi:[1,0,0]\n\t"
        "v_fma_mix_f32 %9, %18, %22, %26 op_sel_hi:[1,0,0]\n\t"
        "v_fma_mix_f32 %10, %15, %23, %27 op_sel_hi:[1,0,0]\n\t"
        "v_fma_mix_f32 %11, %19, %23, %27 op_sel_hi:[1,0,0]\n\t"
        "v_cvt_pk_bf16_f32 %0, %4, %5\n\t"
        "v_cvt_pk_bf16_f32 %1, %6, %7\n\t"
        "v_cvt_pk_bf16_f32 %2, %8, %9\n\t"
        "v_cvt_pk_bf16_f32 %3, %10, %11"
        : "=&v"(o[0][kk]), "=&v"(o[1][kk]), "=&v"(o[2][kk]), "=&v"(o[3][kk]),
          "=&v"(t0), "=&v"(t1), "=&v"(t2), "=&v"(t3), "=&v"(t4), "=&v"(t5), "=&v"(t6), "=&v"(t7)
        : "v"(qa[0]), "v"(qa[1]), "v"(qa[2]), "v"(qa[3]), "v"(qb[0]), "v"(qb[1]), "v"(qb[2]), "v"(qb[3]),
          "v"(S[0]), "v"(S[1]), "v"(S[2]), "v"(S[3]), "v"(C[0]), "v"(C[1]), "v"(C[2]), "v"(C[3]));
}
DI u32x4 dequant_col(const u32x4 (&q)[8], int j, float S, float C) {
    u32x4 o; float t0, t1, t2, t3, t4, t5, t6, t7;
    asm volatile(
        "v_fma_mix_f32 %4, %12, %20, %21 op_sel_hi:[1,0,0]\n\t"
        "v_fma_mix_f32 %5, %13, %20, %21 op_sel_hi:[1,0,0]\n\t"
        "v_fma_mix_f32 %6, %14, %20, %21 op_sel_hi:[1,0,0]\n\t"
        "v_fma_mix_f32 %7, %15, %20, %21 op_sel_hi:[1,0,0]\n\t"
        "v_fma_mix_f32 %8, %16, %20, %21 op_sel_hi:[1,0,0]\n\t"
        "v_fma_mix_f32 %9, %17, %20, %21 op_sel_hi:[1,0,0]\n\t"
        "v_fma_mix_f32 %10, %18, %20, %21 op_sel_hi:[1,0,0]\n\t"
        "v_fma_mix_f32 %11, %19, %20, %21 op_sel_hi:[1,0,0]\n\t"
        "v_cvt_pk_bf16_f32 %0, %4, %5\n\t"
        "v_cvt_pk_bf16_f32 %1, %6, %7\n\t"
        "v_cvt_pk_bf16_f32 %2, %8, %9\n\t"
        "v_cvt_pk_bf16_f32 %3, %10, %11"
        : "=&v"(o[0]), "=&v"(o[1]), "=&v"(o[2]), "=&v"(o[3]),
          "=&v"(t0), "=&v"(t1), "=&v"(t2), "=&v"(t3), "=&v"(t4), "=&v"(t5), "=&v"(t6), "=&v"(t7)
        : "v"(q[0][j]), "v"(q[1][j]), "v"(q[2][j]), "v"(q[3][j]), "v"(q[4][j]), "v"(q[5][j]), "v"(q[6][j]), "v"(q[7][j]), "v"(S), "v"(C));
    return o;
}
DI void make_scales(float (&S)[4], float (&C)[4], const u32x2& s2, const u32x2& z2) {
#pragma unroll
    for (int j = 0; j < 4; ++j) {
        const float s = __uint_as_float((j & 1) ? (s2[j >> 1] & 0xffff0000u) : (s2[j >> 1] << 16));
        const float z = __uint_as_float((j & 1) ? (z2[j >> 1] & 0xffff0000u) : (z2[j >> 1] << 16));
        S[j] = fmul(s, 16777216.0f);
        C[j] = fmul(-z, s);
    }
}
struct PCtx { unsigned vq[8], vx[8], m0a, wa0, wa1; __amdgpu_buffer_rsrc_t srdQ, srdX; };
template <int J> DI void ld_x(const PCtx& c, unsigned sx) { dma16o<(J & 3) * 1024>(c.vx[J], c.srdX, sx); }
template <bool ISSUE, bool NEWS>
DI void pstep(u32x4 (&q)[8], u32x4 (&qn)[8], float (&S)[4], float (&C)[4], u32x2& s2, u32x2& z2, const PCtx& c, int bufoff, int ns) {
    u32x4 o[4];
    const unsigned sq = (unsigned)ns * (BK * QROW), sx = (unsigned)ns * (BK * 2), mb = c.m0a + (unsigned)(ns & 3) * A_STAGE;
    if constexpr (ISSUE) { set_m0(mb); ld_q(qn[0], c.vq[0], c.srdQ, sq); ld_q(qn[1], c.vq[1], c.srdQ, sq); }
    wait_q<ISSUE ? 10 : 8>(q);
#pragma unroll
    for (int kk = 0; kk < 4; ++kk) {
        dequant_pair(o, kk, q[2 * kk], q[2 * kk + 1], S, C);
        if constexpr (ISSUE) { if (kk < 3) { ld_q(qn[2 * kk + 2], c.vq[2 * kk + 2], c.srdQ, sq); ld_q(qn[2 * kk + 3], c.vq[2 * kk + 3], c.srdQ, sq); } }
    }
    if constexpr (ISSUE && SEQ == 2) { ld_x<0>(c, sx); ld_x<1>(c, sx); }
#pragma unroll
    for (int j = 0; j < 4; ++j) *(LAS u32x4*)(uintptr_t)(((j >> 1) ? c.wa1 : c.wa0) + (unsigned)(j * 128 + bufoff)) = o[j];
    if constexpr (ISSUE) { if (SEQ != 2) { ld_x<0>(c, sx); ld_x<1>(c, sx); } ld_x<2>(c, sx); ld_x<3>(c, sx); set_m0(mb + 4096u); }
    if constexpr (NEWS) {
        asm volatile("" : "+v"(s2), "+v"(z2) :: "memory");
        make_scales(S, C, s2, z2);
    } else asm volatile("s_nop 0");
    if constexpr (ISSUE) { ld_x<4>(c, sx); ld_x<5>(c, sx); ld_x<6>(c, sx); ld_x<7>(c, sx); }
    if constexpr (ISSUE) WAIT_V(16); else WAIT_V(0);
}

struct Frag { bf16x8 x[4]; bf16x8 w[8]; };
DI void read_frags(Frag& f, unsigned xa, unsigned w0, unsigned w1, int bufoff) {
    f.x[0] = *(LAS const bf16x8*)(uintptr_t)(xa);
#pragma unroll
    for (int t = 0; t < 8; ++t)
        f.w[t] = *(LAS const bf16x8*)(uintptr_t)(((t & 1) ? w1 : w0) + (unsigned)(t * 2048 + bufoff));
#pragma unroll
    for (int m = 1; m < 4; ++m)
        f.x[m] = *(LAS const bf16x8*)(uintptr_t)(xa + (unsigned)(m * 2048));
}
DI void mma(f32x4 (&acc)[4][8], const Frag& f) {
#pragma unroll
    for (int m = 0; m < 4; ++m)
#pragma unroll
        for (int t = 0; t < 8; ++t)
            acc[m][t] = __builtin_amdgcn_mfma_f32_16x16x32_bf16(f.w[t], f.x[m], acc[m][t], 0, 0, 0);
}
DI void interleave() {
#pragma unroll
    for (int i = 0; i < 12; ++i) {
        __builtin_amdgcn_sched_group_barrier(0x008, 2, 0);
        __builtin_amdgcn_sched_group_barrier(0x100, 1, 0);
    }
    __builtin_amdgcn_sched_group_barrier(0x008, 8, 0);
}

__global__ __launch_bounds__(512, 2)
void w4a16_kernel(const unsigned short* __restrict__ x, const int* __restrict__ qw, const unsigned short* __restrict__ sc,
                  const unsigned short* __restrict__ zr, unsigned short* __restrict__ out, char* __restrict__ ws) {
    __shared__ __attribute__((aligned(1024))) char smem[LDS_TOTAL];
    const unsigned lbase = (unsigned)(uintptr_t)(LAS char*)smem;
    const int lane = threadIdx.x & 63;
    const int wid = __builtin_amdgcn_readfirstlane(threadIdx.x >> 6);
    const int bid = blockIdx.x, xcd = bid & 7, idx = bid >> 3;
    const int tn0 = (xcd >> 1) * 7 + (xcd & 1) * 3;
    const __amdgpu_buffer_rsrc_t srdQ = __builtin_amdgcn_make_buffer_rsrc((void*)qw, (short)0, K_ * N_ * 4, 0x00020000);
    STAMP_DECL;
    STAMP(0);
    if (idx >= 28) {
        if (HELP && wid == 0) {
            const __amdgpu_buffer_rsrc_t srdXh = __builtin_amdgcn_make_buffer_rsrc((void*)x, (short)0, M_ * K_ * 2, 0x00020000);
            const int h = idx - 28;
            const unsigned v = (unsigned)((h * 16 + (lane >> 4)) * QROW + tn0 * 512 + (lane & 15) * 128);
            const unsigned vxh = (unsigned)((xcd * 256 + h * 64 + lane) * XROW);
            if (HELPS) {
                const __amdgpu_buffer_rsrc_t srdSh = __builtin_amdgcn_make_buffer_rsrc((void*)sc, (short)0, 8 * N_ * 2, 0x00020000);
                const __amdgpu_buffer_rsrc_t srdZh = __builtin_amdgcn_make_buffer_rsrc((void*)zr, (short)0, 8 * N_ * 2, 0x00020000);
                const unsigned vsh = lane < 16 ? (unsigned)((2 * h + (lane >> 3)) * N_ * 2 + tn0 * 256 + (lane & 7) * 128) : 0x80000000u;
                dma4(lbase, vsh, srdSh, 0u); dma4(lbase, vsh, srdZh, 0u);
            }
            for (int s = HSTART; s < NIT; ++s) {
                if (HELPX) dma4(lbase, vxh, srdXh, (unsigned)(s * 128));
#pragma unroll
                for (int u = 0; u < 4; ++u) dma4(lbase, v, srdQ, (unsigned)(s * BK + u * 4) * QROW);
            }
            WAIT_V(0);
        }
        STAMP(21);
        STAMP_OUT();
        return;
    }
    int tm, tn;
    if (idx < 24) { tm = idx & 7; tn = tn0 + (xcd & 1) + (idx >> 3); }
    else { tm = (xcd & 1) * 4 + (idx - 24); tn = (xcd >> 1) * 7 + 3; }
    const int m0 = tm * BM, n0 = tn * BN;

    if (wid >= 4) {
        const int pw = wid - 4;
        PCtx c;
        c.srdQ = srdQ;
        c.srdX = __builtin_amdgcn_make_buffer_rsrc((void*)((const char*)x - 4096), (short)0, M_ * K_ * 2 + 4096, 0x00020000);
        const __amdgpu_buffer_rsrc_t srdS = __builtin_amdgcn_make_buffer_rsrc((void*)sc, (short)0, 8 * N_ * 2, 0x00020000);
        const __amdgpu_buffer_rsrc_t srdZ = __builtin_amdgcn_make_buffer_rsrc((void*)zr, (short)0, 8 * N_ * 2, 0x00020000);
        const int kb = 2 * pw + (lane >> 5), nb = lane & 31;
#pragma unroll
        for (int i = 0; i < 8; ++i) c.vq[i] = (unsigned)((kb * 8 + i) * QROW + (n0 + 4 * nb) * 4);
        const unsigned vs = (unsigned)((n0 + 4 * nb) * 2);
        u32x4 q0[8], q1[8]; u32x2 s2, z2;
        ld8(s2, vs, srdS, 0u); ld8(z2, vs, srdZ, 0u);
#pragma unroll
        for (int i = 0; i < 8; ++i) ld_q(q0[i], c.vq[i], c.srdQ, 0u);
#pragma unroll
        for (int j = 0; j < 8; ++j) {
            const int row = pw * 64 + j * 8 + (lane >> 3);
            c.vx[j] = (unsigned)((m0 + row) * XROW + (((lane & 7) ^ ((row >> 1) & 7)) * 16) + 4096 - (j & 3) * 1024);
        }
        c.m0a = lbase + LDS_A + pw * 8192;
        set_m0(c.m0a);
        asm volatile("s_nop 0");
        ld_x<0>(c, 0u); ld_x<1>(c, 0u); ld_x<2>(c, 0u); ld_x<3>(c, 0u);
        set_m0(c.m0a + 4096u);
        asm volatile("s_nop 0");
        ld_x<4>(c, 0u); ld_x<5>(c, 0u); ld_x<6>(c, 0u); ld_x<7>(c, 0u);
        const int rb = 32 * (nb >> 3) + 16 * (nb & 1) + 4 * ((nb >> 1) & 3);
        const int f = ((rb >> 1) & 7) ^ ((rb >> 4) & 1);
        c.wa0 = lbase + (unsigned)(LDS_B + rb * 128 + ((kb ^ f) * 16));
        c.wa1 = c.wa0 ^ 16u;
        STAMP(1);
        float S[4], C[4];
        asm volatile("s_waitcnt vmcnt(16)" : "+v"(s2), "+v"(z2) :: "memory");
        make_scales(S, C, s2, z2);
        for (int b = 0; b < NIT - 2; b += 2) {
            ld8(s2, vs, srdS, (unsigned)((b / 2 + 1) * N_ * 2)); ld8(z2, vs, srdZ, (unsigned)((b / 2 + 1) * N_ * 2));
            pstep<true, false>(q0, q1, S, C, s2, z2, c, 0, b + 1);
            WAIT_L0();
            BAR();
            PSTAMP(2 + b);
            pstep<true, true>(q1, q0, S, C, s2, z2, c, B_STAGE, b + 2);
            WAIT_L0();
            BAR();
            PSTAMP(3 + b);
        }
        pstep<true, false>(q0, q1, S, C, s2, z2, c, 0, NIT - 1);
        WAIT_L0();
        BAR();
        PSTAMP(16);
        pstep<false, false>(q1, q0, S, C, s2, z2, c, B_STAGE, 0);
        WAIT_L0();
        BAR();
        STAMP(17);
        BAR();
    } else {
        const int w = wid;
        {
            constexpr int NX0 = CX0 ? 2048 : 0, NQ = CQ * 1024, NXG = (CQ - CX0) * 256, TOT = NX0 + NQ + NXG, LPW = (TOT + 111) / 112;
            static_assert(LPW <= 64, "one load per wave");
            const int l = lane * 112 + idx * 4 + w;
            if (TOT > 0 && l < TOT) {
                int lq = l - NX0;
                const char* p;
                if (lq < 0) p = (const char*)x + (size_t)l * XROW;
                else if (lq < NQ) p = (const char*)qw + (size_t)(lq >> 4) * QROW + tn0 * 512 + (lq & 15) * 128;
                else { const int lg = lq - NQ; p = (const char*)x + (size_t)(xcd * 256 + (lg & 255)) * XROW + (CX0 + (lg >> 8)) * 128; }
                const unsigned v = *(const volatile unsigned*)p;
                asm volatile("" :: "v"(v));
            }
        }
        const int fr = lane & 15, fq = lane >> 4;
        const unsigned sw = (unsigned)((fq ^ (fr >> 1)) * 16);
        unsigned xa0 = lbase + (unsigned)(LDS_A + (w * 64 + fr) * 128) + sw;
        unsigned w00 = lbase + (unsigned)(LDS_B + fr * 128) + sw;
        unsigned w01 = w00 ^ 16u, w10 = w00 ^ 64u, w11 = w00 ^ 80u;
        asm volatile("" : "+v"(w00), "+v"(w01), "+v"(w10), "+v"(w11));
        f32x4 acc[4][8];
#pragma unroll
        for (int m = 0; m < 4; ++m)
#pragma unroll
            for (int t = 0; t < 8; ++t) { acc[m][t] = (f32x4){0.f, 0.f, 0.f, 0.f}; asm volatile("" : "+v"(acc[m][t])); }
        Frag f0, f1;
        asm volatile("" :: "s"(out));
        BAR();
        STAMP(2);
        read_frags(f0, xa0, w00, w01, 0);
        for (int it = 0; it < NIT; it += 2) {
            unsigned xe = xa0 + (unsigned)((it & 2) * A_STAGE);
            unsigned xn = xa0 + (unsigned)(((it + 2) & 2) * A_STAGE);
            asm volatile("" : "+v"(xe), "+v"(xn));
            SB0();
            read_frags(f1, xe ^ 64u, w10, w11, 0);
            mma(acc, f0);
            interleave();
            SB0();
            WAIT_L0();
            BAR();
            LSTAMP(3 + it);
            SB0();
            read_frags(f0, xe + A_STAGE, w00, w01, B_STAGE);
            mma(acc, f1);
            interleave();
            SB0();
            read_frags(f1, (xe ^ 64u) + A_STAGE, w10, w11, B_STAGE);
            mma(acc, f0);
            interleave();
            SB0();
            WAIT_L0();
            BAR();
            LSTAMP(4 + it);
            SB0();
            read_frags(f0, xn, w00, w01, 0);
            mma(acc, f1);
            interleave();
            SB0();
        }
        STAMP(19);
        const unsigned eb = lbase + (unsigned)(A_STAGE + w * (64 * OROW));
        const unsigned ew = eb + (unsigned)(fr * OROW + fq * 16);
        const unsigned er = eb + (unsigned)(fq * OROW + fr * 16);
        const __amdgpu_buffer_rsrc_t srdO = __builtin_amdgcn_make_buffer_rsrc((void*)out, (short)0, M_ * N_ * 2, 0x00020000);
        const int ovo = fq * (N_ * 2) + fr * 16;
        const int oso = ((m0 + w * 64) * N_ + n0) * 2;
#pragma unroll
        for (int m = 0; m < 4; ++m) {
#pragma unroll
            for (int pp = 0; pp < 4; ++pp) {
                u32x4 v;
                v[0] = cvt_pk_bf16(acc[m][2 * pp][0], acc[m][2 * pp][1]);
                v[1] = cvt_pk_bf16(acc[m][2 * pp][2], acc[m][2 * pp][3]);
                v[2] = cvt_pk_bf16(acc[m][2 * pp + 1][0], acc[m][2 * pp + 1][1]);
                v[3] = cvt_pk_bf16(acc[m][2 * pp + 1][2], acc[m][2 * pp + 1][3]);
                *(LAS u32x4*)(uintptr_t)(ew + (unsigned)(m * 16 * OROW + pp * 64)) = v;
            }
#pragma unroll
            for (int i = 0; i < 4; ++i) {
                const u32x4 v = *(LAS const u32x4*)(uintptr_t)(er + (unsigned)((m * 16 + i * 4) * OROW));
                __builtin_amdgcn_raw_buffer_store_b128(v, srdO, ovo, oso + (m * 16 + i * 4) * (N_ * 2), STAUX);
            }
        }
        STAMP(20);
        STAMP(21);
    }
    STAMP_OUT();
}
}

extern "C" void kernel_launch(void* const* d_in, const int* in_sizes, int n_in,
                              void* d_out, int out_size, void* d_ws, size_t ws_size,
                              hipStream_t stream) {
    w4a16_kernel<<<256, 512, 0, stream>>>((const unsigned short*)d_in[0], (const int*)d_in[1],
        (const unsigned short*)d_in[2], (const unsigned short*)d_in[3], (unsigned short*)d_out, (char*)d_ws);
}
```

```cpp
#define DIAG 0
#include <hip/hip_runtime.h>
#include <hip/hip_bf16.h>
#include <type_traits>

typedef short bf16x8 __attribute__((ext_vector_type(8)));
typedef float f32x4 __attribute__((ext_vector_type(4)));
typedef unsigned u32x4 __attribute__((ext_vector_type(4)));
typedef unsigned u32x2 __attribute__((ext_vector_type(2)));
#define LAS __attribute__((address_space(3)))
#define DI __device__ __forceinline__

#define QFIRST 4
#define CQ 1
#define CX0 1
#define HELPX 1
#define HELPS 1
#define SEQ 2
#define HELP 1
#define HSTART CQ
#define PFX 0
#define XROT 1
#define STAUX 2

namespace {
constexpr int M_ = 2048, N_ = 3584, K_ = 1024;
constexpr int BM = 256, BN = 128, BK = 64, NIT = K_ / BK;
constexpr int A_STAGE = BM * BK * 2;
constexpr int B_STAGE = BN * BK * 2;
constexpr int LDS_A = 0, LDS_B = 4 * A_STAGE;
constexpr int LDS_TOTAL = LDS_B + 2 * B_STAGE;
constexpr int QROW = N_ * 4;
constexpr int XROW = K_ * 2;
constexpr int OROW = 272;

#define STAMP(i)
#define STAMP_DECL
#define STAMP_OUT()
#define LSTAMP(i) STAMP(i)
#define PSTAMP(i) LSTAMP(i)
#define WAIT_V(n) asm volatile("s_waitcnt vmcnt(%0)" ::"n"(n) : "memory")
#define WAIT_L0() asm volatile("s_waitcnt lgkmcnt(0)" ::: "memory")
#define BAR() do { asm volatile("" ::: "memory"); __builtin_amdgcn_s_barrier(); asm volatile("" ::: "memory"); } while (0)
#define SB0() __builtin_amdgcn_sched_barrier(0)

DI unsigned cvt_pk_bf16(float lo, float hi) { unsigned r; asm("v_cvt_pk_bf16_f32 %0, %1, %2" : "=v"(r) : "v"(lo), "v"(hi)); return r; }
DI float fmul(float a, float b) { float r; asm volatile("v_mul_f32 %0, %1, %2" : "=v"(r) : "v"(a), "v"(b)); return r; }
DI void ld_q(u32x4& q, unsigned vq, __amdgpu_buffer_rsrc_t srd, unsigned so) {
    asm volatile("buffer_load_dwordx4 %0, %1, %2, %3 offen" : "=v"(q) : "v"(vq), "s"(srd), "s"(so) : "memory");
}
DI void ld8(u32x2& q, unsigned v, __amdgpu_buffer_rsrc_t srd, unsigned so) {
    asm volatile("buffer_load_dwordx2 %0, %1, %2, %3 offen" : "=v"(q) : "v"(v), "s"(srd), "s"(so) : "memory");
}
DI void dma16(unsigned m0v, unsigned v, __amdgpu_buffer_rsrc_t srd, unsigned so) {
    asm volatile("s_mov_b32 m0, %0\n\ts_nop 0\n\tbuffer_load_dwordx4 %1, %2, %3 offen lds" :: "s"(m0v), "v"(v), "s"(srd), "s"(so) : "memory");
}
DI void set_m0(unsigned m0v) { asm volatile("s_mov_b32 m0, %0" :: "s"(m0v) : "memory"); }
template <int OFF> DI void dma16o(unsigned v, __amdgpu_buffer_rsrc_t srd, unsigned so) {
    asm volatile("buffer_load_dwordx4 %0, %1, %2 offen offset:%3 lds" :: "v"(v), "s"(srd), "s"(so), "n"(OFF) : "memory");
}
DI void dma4(unsigned m0v, unsigned v, __amdgpu_buffer_rsrc_t srd, unsigned so) {
    asm volatile("s_mov_b32 m0, %0\n\ts_nop 0\n\tbuffer_load_dword %1, %2, %3 offen lds" :: "s"(m0v), "v"(v), "s"(srd), "s"(so) : "memory");
}
template <int W> DI void wait_q(u32x4 (&q)[8]) {
    asm volatile("s_waitcnt vmcnt(%8)"
                 : "+v"(q[0]), "+v"(q[1]), "+v"(q[2]), "+v"(q[3]), "+v"(q[4]), "+v"(q[5]), "+v"(q[6]), "+v"(q[7]) : "n"(W) : "memory");
}
DI void dequant_pair(u32x4 (&o)[4], int kk, const u32x4& qa, const u32x4& qb, const float (&S)[4], const float (&C)[4]) {
    float t0, t1, t2, t3, t4, t5, t6, t7;
    asm volatile(
        "v_fma_mix_f32 %4, %12, %20, %24 op_sel_hi:[1,0,0]\n\t"
        "v_fma_mix_f32 %5, %16, %20, %24 op_sel_hi:[1,0,0]\n\t"
        "v_fma_mix_f32 %6, %13, %21, %25 op_sel_hi:[1,0,0]\n\t"
        "v_fma_mix_f32 %7, %17, %21, %25 op_sel_hi:[1,0,0]\n\t"
        "v_fma_mix_f32 %8, %14, %22, %26 op_sel_hi:[1,0,0]\n\t"
        "v_fma_mix_f32 %9, %18, %22, %26 op_sel_hi:[1,0,0]\n\t"
        "v_fma_mix_f32 %10, %15, %23, %27 op_sel_hi:[1,0,0]\n\t"
        "v_fma_mix_f32 %11, %19, %23, %27 op_sel_hi:[1,0,0]\n\t"
        "v_cvt_pk_bf16_f32 %0, %4, %5\n\t"
        "v_cvt_pk_bf16_f32 %1, %6, %7\n\t"
        "v_cvt_pk_bf16_f32 %2, %8, %9\n\t"
        "v_cvt_pk_bf16_f32 %3, %10, %11"
        : "=&v"(o[0][kk]), "=&v"(o[1][kk]), "=&v"(o[2][kk]), "=&v"(o[3][kk]),
          "=&v"(t0), "=&v"(t1), "=&v"(t2), "=&v"(t3), "=&v"(t4), "=&v"(t5), "=&v"(t6), "=&v"(t7)
        : "v"(qa[0]), "v"(qa[1]), "v"(qa[2]), "v"(qa[3]), "v"(qb[0]), "v"(qb[1]), "v"(qb[2]), "v"(qb[3]),
          "v"(S[0]), "v"(S[1]), "v"(S[2]), "v"(S[3]), "v"(C[0]), "v"(C[1]), "v"(C[2]), "v"(C[3]));
}
DI u32x4 dequant_col(const u32x4 (&q)[8], int j, float S, float C) {
    u32x4 o; float t0, t1, t2, t3, t4, t5, t6, t7;
    asm volatile(
        "v_fma_mix_f32 %4, %12, %20, %21 op_sel_hi:[1,0,0]\n\t"
        "v_fma_mix_f32 %5, %13, %20, %21 op_sel_hi:[1,0,0]\n\t"
        "v_fma_mix_f32 %6, %14, %20, %21 op_sel_hi:[1,0,0]\n\t"
        "v_fma_mix_f32 %7, %15, %20, %21 op_sel_hi:[1,0,0]\n\t"
        "v_fma_mix_f32 %8, %16, %20, %21 op_sel_hi:[1,0,0]\n\t"
        "v_fma_mix_f32 %9, %17, %20, %21 op_sel_hi:[1,0,0]\n\t"
        "v_fma_mix_f32 %10, %18, %20, %21 op_sel_hi:[1,0,0]\n\t"
        "v_fma_mix_f32 %11, %19, %20, %21 op_sel_hi:[1,0,0]\n\t"
        "v_cvt_pk_bf16_f32 %0, %4, %5\n\t"
        "v_cvt_pk_bf16_f32 %1, %6, %7\n\t"
        "v_cvt_pk_bf16_f32 %2, %8, %9\n\t"
        "v_cvt_pk_bf16_f32 %3, %10, %11"
        : "=&v"(o[0]), "=&v"(o[1]), "=&v"(o[2]), "=&v"(o[3]),
          "=&v"(t0), "=&v"(t1), "=&v"(t2), "=&v"(t3), "=&v"(t4), "=&v"(t5), "=&v"(t6), "=&v"(t7)
        : "v"(q[0][j]), "v"(q[1][j]), "v"(q[2][j]), "v"(q[3][j]), "v"(q[4][j]), "v"(q[5][j]), "v"(q[6][j]), "v"(q[7][j]), "v"(S), "v"(C));
    return o;
}
DI void make_scales(float (&S)[4], float (&C)[4], const u32x2& s2, const u32x2& z2) {
#pragma unroll
    for (int j = 0; j < 4; ++j) {
        const float s = __uint_as_float((j & 1) ? (s2[j >> 1] & 0xffff0000u) : (s2[j >> 1] << 16));
        const float z = __uint_as_float((j & 1) ? (z2[j >> 1] & 0xffff0000u) : (z2[j >> 1] << 16));
        S[j] = fmul(s, 16777216.0f);
        C[j] = fmul(-z, s);
    }
}
struct PCtx { unsigned vq[8], vx[8], m0a, wa0, wa1; __amdgpu_buffer_rsrc_t srdQ, srdX; };
template <int J> DI void ld_x(const PCtx& c, unsigned sx) { dma16o<(J & 3) * 1024>(c.vx[J], c.srdX, sx); }
template <bool ISSUE, bool NEWS>
DI void pstep(u32x4 (&q)[8], u32x4 (&qn)[8], float (&S)[4], float (&C)[4], u32x2& s2, u32x2& z2, const PCtx& c, int bufoff, int ns) {
    u32x4 o[4];
    const unsigned sq = (unsigned)ns * (BK * QROW), sx = (unsigned)ns * (BK * 2), mb = c.m0a + (unsigned)(ns & 3) * A_STAGE;
    if constexpr (ISSUE) { set_m0(mb); ld_q(qn[0], c.vq[0], c.srdQ, sq); ld_q(qn[1], c.vq[1], c.srdQ, sq); }
    wait_q<ISSUE ? 10 : 8>(q);
#pragma unroll
    for (int kk = 0; kk < 4; ++kk) {
        dequant_pair(o, kk, q[2 * kk], q[2 * kk + 1], S, C);
        if constexpr (ISSUE) { if (kk < 3) { ld_q(qn[2 * kk + 2], c.vq[2 * kk + 2], c.srdQ, sq); ld_q(qn[2 * kk + 3], c.vq[2 * kk + 3], c.srdQ, sq); } }
    }
    if constexpr (ISSUE && SEQ == 2) { ld_x<0>(c, sx); ld_x<1>(c, sx); }
#pragma unroll
    for (int j = 0; j < 4; ++j) *(LAS u32x4*)(uintptr_t)(((j >> 1) ? c.wa1 : c.wa0) + (unsigned)(j * 128 + bufoff)) = o[j];
    if constexpr (ISSUE) { if (SEQ != 2) { ld_x<0>(c, sx); ld_x<1>(c, sx); } ld_x<2>(c, sx); ld_x<3>(c, sx); set_m0(mb + 4096u); }
    if constexpr (NEWS) {
        asm volatile("" : "+v"(s2), "+v"(z2) :: "memory");
        make_scales(S, C, s2, z2);
    } else asm volatile("s_nop 0");
    if constexpr (ISSUE) { ld_x<4>(c, sx); ld_x<5>(c, sx); ld_x<6>(c, sx); ld_x<7>(c, sx); }
    if constexpr (ISSUE) WAIT_V(16); else WAIT_V(0);
}

struct Frag { bf16x8 x[4]; bf16x8 w[8]; };
DI void read_frags(Frag& f, unsigned xa, unsigned w0, unsigned w1, int bufoff) {
    f.x[0] = *(LAS const bf16x8*)(uintptr_t)(xa);
#pragma unroll
    for (int t = 0; t < 8; ++t)
        f.w[t] = *(LAS const bf16x8*)(uintptr_t)(((t & 1) ? w1 : w0) + (unsigned)(t * 2048 + bufoff));
#pragma unroll
    for (int m = 1; m < 4; ++m)
        f.x[m] = *(LAS const bf16x8*)(uintptr_t)(xa + (unsigned)(m * 2048));
}
DI void mma(f32x4 (&acc)[4][8], const Frag& f) {
#pragma unroll
    for (int m = 0; m < 4; ++m)
#pragma unroll
        for (int t = 0; t < 8; ++t)
            acc[m][t] = __builtin_amdgcn_mfma_f32_16x16x32_bf16(f.w[t], f.x[m], acc[m][t], 0, 0, 0);
}
DI void interleave() {
#pragma unroll
    for (int i = 0; i < 12; ++i) {
        __builtin_amdgcn_sched_group_barrier(0x008, 2, 0);
        __builtin_amdgcn_sched_group_barrier(0x100, 1, 0);
    }
    __builtin_amdgcn_sched_group_barrier(0x008, 8, 0);
}

__global__ __launch_bounds__(512, 2)
void w4a16_kernel(const unsigned short* __restrict__ x, const int* __restrict__ qw, const unsigned short* __restrict__ sc,
                  const unsigned short* __restrict__ zr, unsigned short* __restrict__ out, char* __restrict__ ws) {
    __shared__ __attribute__((aligned(1024))) char smem[LDS_TOTAL];
    const unsigned lbase = (unsigned)(uintptr_t)(LAS char*)smem;
    const int lane = threadIdx.x & 63;
    const int wid = __builtin_amdgcn_readfirstlane(threadIdx.x >> 6);
    const int bid = blockIdx.x, xcd = bid & 7, idx = bid >> 3;
    const int tn0 = (xcd >> 1) * 7 + (xcd & 1) * 3;
    const __amdgpu_buffer_rsrc_t srdQ = __builtin_amdgcn_make_buffer_rsrc((void*)qw, (short)0, K_ * N_ * 4, 0x00020000);
    STAMP_DECL;
    STAMP(0);
    unsigned long long dg0, dg2, dg19, dg21, dc2, dc19;
#define RT(v) asm volatile("s_memrealtime %0\n\ts_waitcnt lgkmcnt(0)" : "=s"(v) :: "memory")
#define CT(v) asm volatile("s_memtime %0\n\ts_waitcnt lgkmcnt(0)" : "=s"(v) :: "memory")
    RT(dg0);
    if (idx >= 28) {
        if (HELP && wid == 0) {
            const __amdgpu_buffer_rsrc_t srdXh = __builtin_amdgcn_make_buffer_rsrc((void*)x, (short)0, M_ * K_ * 2, 0x00020000);
            const int h = idx - 28;
            const unsigned v = (unsigned)((h * 16 + (lane >> 4)) * QROW + tn0 * 512 + (lane & 15) * 128);
            const unsigned vxh = (unsigned)((xcd * 256 + h * 64 + lane) * XROW);
            if (HELPS) {
                const __amdgpu_buffer_rsrc_t srdSh = __builtin_amdgcn_make_buffer_rsrc((void*)sc, (short)0, 8 * N_ * 2, 0x00020000);
                const __amdgpu_buffer_rsrc_t srdZh = __builtin_amdgcn_make_buffer_rsrc((void*)zr, (short)0, 8 * N_ * 2, 0x00020000);
                const unsigned vsh = lane < 16 ? (unsigned)((2 * h + (lane >> 3)) * N_ * 2 + tn0 * 256 + (lane & 7) * 128) : 0x80000000u;
                dma4(lbase, vsh, srdSh, 0u); dma4(lbase, vsh, srdZh, 0u);
            }
            for (int s = HSTART; s < NIT; ++s) {
                if (HELPX) dma4(lbase, vxh, srdXh, (unsigned)(s * 128));
#pragma unroll
                for (int u = 0; u < 4; ++u) dma4(lbase, v, srdQ, (unsigned)(s * BK + u * 4) * QROW);
            }
            WAIT_V(0);
            if (DIAG == 6) { RT(dg21); unsigned long long now; do { __builtin_amdgcn_s_sleep(4); RT(now); } while (now - dg0 < 3000 + 8 * (dg21 - dg0)); }
        }
        return;
    }
    int tm, tn;
    if (idx < 24) { tm = idx & 7; tn = tn0 + (xcd & 1) + (idx >> 3); }
    else { tm = (xcd & 1) * 4 + (idx - 24); tn = (xcd >> 1) * 7 + 3; }
    const int m0 = tm * BM, n0 = tn * BN;

    if (wid >= 4) {
        const int pw = wid - 4;
        PCtx c;
        c.srdQ = srdQ;
        c.srdX = __builtin_amdgcn_make_buffer_rsrc((void*)((const char*)x - 4096), (short)0, M_ * K_ * 2 + 4096, 0x00020000);
        const __amdgpu_buffer_rsrc_t srdS = __builtin_amdgcn_make_buffer_rsrc((void*)sc, (short)0, 8 * N_ * 2, 0x00020000);
        const __amdgpu_buffer_rsrc_t srdZ = __builtin_amdgcn_make_buffer_rsrc((void*)zr, (short)0, 8 * N_ * 2, 0x00020000);
        const int kb = 2 * pw + (lane >> 5), nb = lane & 31;
#pragma unroll
        for (int i = 0; i < 8; ++i) c.vq[i] = (unsigned)((kb * 8 + i) * QROW + (n0 + 4 * nb) * 4);
        const unsigned vs = (unsigned)((n0 + 4 * nb) * 2);
        u32x4 q0[8], q1[8]; u32x2 s2, z2;
        ld8(s2, vs, srdS, 0u); ld8(z2, vs, srdZ, 0u);
#pragma unroll
        for (int i = 0; i < 8; ++i) ld_q(q0[i], c.vq[i], c.srdQ, 0u);
#pragma unroll
        for (int j = 0; j < 8; ++j) {
            const int row = pw * 64 + j * 8 + (lane >> 3);
            c.vx[j] = (unsigned)((m0 + row) * XROW + (((lane & 7) ^ ((row >> 1) & 7)) * 16) + 4096 - (j & 3) * 1024);
        }
        c.m0a = lbase + LDS_A + pw * 8192;
        set_m0(c.m0a);
        asm volatile("s_nop 0");
        ld_x<0>(c, 0u); ld_x<1>(c, 0u); ld_x<2>(c, 0u); ld_x<3>(c, 0u);
        set_m0(c.m0a + 4096u);
        asm volatile("s_nop 0");
        ld_x<4>(c, 0u); ld_x<5>(c, 0u); ld_x<6>(c, 0u); ld_x<7>(c, 0u);
        const int rb = 32 * (nb >> 3) + 16 * (nb & 1) + 4 * ((nb >> 1) & 3);
        const int f = ((rb >> 1) & 7) ^ ((rb >> 4) & 1);
        c.wa0 = lbase + (unsigned)(LDS_B + rb * 128 + ((kb ^ f) * 16));
        c.wa1 = c.wa0 ^ 16u;
        STAMP(1);
        float S[4], C[4];
        asm volatile("s_waitcnt vmcnt(16)" : "+v"(s2), "+v"(z2) :: "memory");
        make_scales(S, C, s2, z2);
        for (int b = 0; b < NIT - 2; b += 2) {
            ld8(s2, vs, srdS, (unsigned)((b / 2 + 1) * N_ * 2)); ld8(z2, vs, srdZ, (unsigned)((b / 2 + 1) * N_ * 2));
            pstep<true, false>(q0, q1, S, C, s2, z2, c, 0, b + 1);
            WAIT_L0();
            BAR();
            PSTAMP(2 + b);
            pstep<true, true>(q1, q0, S, C, s2, z2, c, B_STAGE, b + 2);
            WAIT_L0();
            BAR();
            PSTAMP(3 + b);
        }
        pstep<true, false>(q0, q1, S, C, s2, z2, c, 0, NIT - 1);
        WAIT_L0();
        BAR();
        PSTAMP(16);
        pstep<false, false>(q1, q0, S, C, s2, z2, c, B_STAGE, 0);
        WAIT_L0();
        BAR();
        STAMP(17);
        BAR();
    } else {
        const int w = wid;
        {
            constexpr int NX0 = CX0 ? 2048 : 0, NQ = CQ * 1024, NXG = (CQ - CX0) * 256, TOT = NX0 + NQ + NXG, LPW = (TOT + 111) / 112;
            static_assert(LPW <= 64, "one load per wave");
            const int l = lane * 112 + idx * 4 + w;
            if (TOT > 0 && l < TOT) {
                int lq = l - NX0;
                const char* p;
                if (lq < 0) p = (const char*)x + (size_t)l * XROW;
                else if (lq < NQ) p = (const char*)qw + (size_t)(lq >> 4) * QROW + tn0 * 512 + (lq & 15) * 128;
                else { const int lg = lq - NQ; p = (const char*)x + (size_t)(xcd * 256 + (lg & 255)) * XROW + (CX0 + (lg >> 8)) * 128; }
                const unsigned v = *(const volatile unsigned*)p;
                asm volatile("" :: "v"(v));
            }
        }
        const int fr = lane & 15, fq = lane >> 4;
        const unsigned sw = (unsigned)((fq ^ (fr >> 1)) * 16);
        unsigned xa0 = lbase + (unsigned)(LDS_A + (w * 64 + fr) * 128) + sw;
        unsigned w00 = lbase + (unsigned)(LDS_B + fr * 128) + sw;
        unsigned w01 = w00 ^ 16u, w10 = w00 ^ 64u, w11 = w00 ^ 80u;
        asm volatile("" : "+v"(w00), "+v"(w01), "+v"(w10), "+v"(w11));
        f32x4 acc[4][8];
#pragma unroll
        for (int m = 0; m < 4; ++m)
#pragma unroll
            for (int t = 0; t < 8; ++t) { acc[m][t] = (f32x4){0.f, 0.f, 0.f, 0.f}; asm volatile("" : "+v"(acc[m][t])); }
        Frag f0, f1;
        asm volatile("" :: "s"(out));
        BAR();
        RT(dg2); CT(dc2);
        read_frags(f0, xa0, w00, w01, 0);
        for (int it = 0; it < NIT; it += 2) {
            unsigned xe = xa0 + (unsigned)((it & 2) * A_STAGE);
            unsigned xn = xa0 + (unsigned)(((it + 2) & 2) * A_STAGE);
            asm volatile("" : "+v"(xe), "+v"(xn));
            SB0();
            read_frags(f1, xe ^ 64u, w10, w11, 0);
            mma(acc, f0);
            interleave();
            SB0();
            WAIT_L0();
            BAR();
            LSTAMP(3 + it);
            SB0();
            read_frags(f0, xe + A_STAGE, w00, w01, B_STAGE);
            mma(acc, f1);
            interleave();
            SB0();
            read_frags(f1, (xe ^ 64u) + A_STAGE, w10, w11, B_STAGE);
            mma(acc, f0);
            interleave();
            SB0();
            WAIT_L0();
            BAR();
            LSTAMP(4 + it);
            SB0();
            read_frags(f0, xn, w00, w01, 0);
            mma(acc, f1);
            interleave();
            SB0();
        }
        SB0(); RT(dg19); CT(dc19); SB0();
        const unsigned eb = lbase + (unsigned)(A_STAGE + w * (64 * OROW));
        const unsigned ew = eb + (unsigned)(fr * OROW + fq * 16);
        const unsigned er = eb + (unsigned)(fq * OROW + fr * 16);
        const __amdgpu_buffer_rsrc_t srdO = __builtin_amdgcn_make_buffer_rsrc((void*)out, (short)0, M_ * N_ * 2, 0x00020000);
        const int ovo = fq * (N_ * 2) + fr * 16;
        const int oso = ((m0 + w * 64) * N_ + n0) * 2;
#pragma unroll
        for (int m = 0; m < 4; ++m) {
#pragma unroll
            for (int pp = 0; pp < 4; ++pp) {
                u32x4 v;
                v[0] = cvt_pk_bf16(acc[m][2 * pp][0], acc[m][2 * pp][1]);
                v[1] = cvt_pk_bf16(acc[m][2 * pp][2], acc[m][2 * pp][3]);
                v[2] = cvt_pk_bf16(acc[m][2 * pp + 1][0], acc[m][2 * pp + 1][1]);
                v[3] = cvt_pk_bf16(acc[m][2 * pp + 1][2], acc[m][2 * pp + 1][3]);
                *(LAS u32x4*)(uintptr_t)(ew + (unsigned)(m * 16 * OROW + pp * 64)) = v;
            }
#pragma unroll
            for (int i = 0; i < 4; ++i) {
                const u32x4 v = *(LAS const u32x4*)(uintptr_t)(er + (unsigned)((m * 16 + i * 4) * OROW));
                __builtin_amdgcn_raw_buffer_store_b128(v, srdO, ovo, oso + (m * 16 + i * 4) * (N_ * 2), STAUX);
            }
        }
        STAMP(20);
        WAIT_V(0);
        RT(dg21);
        { const unsigned long long q = DIAG == 1 ? dg2 - dg0 : DIAG == 2 ? dg19 - dg2 : DIAG == 3 ? dg21 - dg19 : DIAG == 4 ? (dc19 - dc2) / 24 : DIAG == 5 ? dg21 - dg0 : 0;
          unsigned long long now; do { __builtin_amdgcn_s_sleep(4); RT(now); } while (now - dg0 < 3000 + 8 * q); }
    }
    STAMP_OUT();
}
}

extern "C" void kernel_launch(void* const* d_in, const int* in_sizes, int n_in,
                              void* d_out, int out_size, void* d_ws, size_t ws_size,
                              hipStream_t stream) {
    w4a16_kernel<<<256, 512, 0, stream>>>((const unsigned short*)d_in[0], (const int*)d_in[1],
        (const unsigned short*)d_in[2], (const unsigned short*)d_in[3], (unsigned short*)d_out, (char*)d_ws);
}
```

```cpp
#define DIAG 0
#include <hip/hip_runtime.h>
#include <hip/hip_bf16.h>
#include <type_traits>

typedef short bf16x8 __attribute__((ext_vector_type(8)));
typedef float f32x4 __attribute__((ext_vector_type(4)));
typedef unsigned u32x4 __attribute__((ext_vector_type(4)));
typedef unsigned u32x2 __attribute__((ext_vector_type(2)));
#define LAS __attribute__((address_space(3)))
#define DI __device__ __forceinline__

#define QFIRST 4
#define CQ 1
#define CX0 1
#define HELPX 1
#define HELPS 1
#define SEQ 2
#define HELP 1
#define HSTART CQ
#define PFX 0
#define XROT 1
#define STAUX 2

namespace {
constexpr int M_ = 2048, N_ = 3584, K_ = 1024;
constexpr int BM = 256, BN = 128, BK = 64, NIT = K_ / BK;
constexpr int A_STAGE = BM * BK * 2;
constexpr int B_STAGE = BN * BK * 2;
constexpr int LDS_A = 0, LDS_B = 4 * A_STAGE;
constexpr int LDS_TOTAL = LDS_B + 2 * B_STAGE;
constexpr int QROW = N_ * 4;
constexpr int XROW = K_ * 2;
constexpr int OROW = 272;

#define STAMP(i)
#define STAMP_DECL
#define STAMP_OUT()
#define LSTAMP(i) STAMP(i)
#define PSTAMP(i) LSTAMP(i)
#define WAIT_V(n) asm volatile("s_waitcnt vmcnt(%0)" ::"n"(n) : "memory")
#define WAIT_L0() asm volatile("s_waitcnt lgkmcnt(0)" ::: "memory")
#define BAR() do { asm volatile("" ::: "memory"); __builtin_amdgcn_s_barrier(); asm volatile("" ::: "memory"); } while (0)
#define SB0() __builtin_amdgcn_sched_barrier(0)

DI unsigned cvt_pk_bf16(float lo, float hi) { unsigned r; asm("v_cvt_pk_bf16_f32 %0, %1, %2" : "=v"(r) : "v"(lo), "v"(hi)); return r; }
DI float fmul(float a, float b) { float r; asm volatile("v_mul_f32 %0, %1, %2" : "=v"(r) : "v"(a), "v"(b)); return r; }
DI void ld_q(u32x4& q, unsigned vq, __amdgpu_buffer_rsrc_t srd, unsigned so) {
    asm volatile("buffer_load_dwordx4 %0, %1, %2, %3 offen" : "=v"(q) : "v"(vq), "s"(srd), "s"(so) : "memory");
}
DI void ld8(u32x2& q, unsigned v, __amdgpu_buffer_rsrc_t srd, unsigned so) {
    asm volatile("buffer_load_dwordx2 %0, %1, %2, %3 offen" : "=v"(q) : "v"(v), "s"(srd), "s"(so) : "memory");
}
DI void dma16(unsigned m0v, unsigned v, __amdgpu_buffer_rsrc_t srd, unsigned so) {
    asm volatile("s_mov_b32 m0, %0\n\ts_nop 0\n\tbuffer_load_dwordx4 %1, %2, %3 offen lds" :: "s"(m0v), "v"(v), "s"(srd), "s"(so) : "memory");
}
DI void set_m0(unsigned m0v) { asm volatile("s_mov_b32 m0, %0" :: "s"(m0v) : "memory"); }
template <int OFF> DI void dma16o(unsigned v, __amdgpu_buffer_rsrc_t srd, unsigned so) {
    asm volatile("buffer_load_dwordx4 %0, %1, %2 offen offset:%3 lds" :: "v"(v), "s"(srd), "s"(so), "n"(OFF) : "memory");
}
DI void dma4(unsigned m0v, unsigned v, __amdgpu_buffer_rsrc_t srd, unsigned so) {
    asm volatile("s_mov_b32 m0, %0\n\ts_nop 0\n\tbuffer_load_dword %1, %2, %3 offen lds" :: "s"(m0v), "v"(v), "s"(srd), "s"(so) : "memory");
}
template <int W> DI void wait_q(u32x4 (&q)[8]) {
    asm volatile("s_waitcnt vmcnt(%8)"
                 : "+v"(q[0]), "+v"(q[1]), "+v"(q[2]), "+v"(q[3]), "+v"(q[4]), "+v"(q[5]), "+v"(q[6]), "+v"(q[7]) : "n"(W) : "memory");
}
DI void dequant_pair(u32x4 (&o)[4], int kk, const u32x4& qa, const u32x4& qb, const float (&S)[4], const float (&C)[4]) {
    float t0, t1, t2, t3, t4, t5, t6, t7;
    asm volatile(
        "v_fma_mix_f32 %4, %12, %20, %24 op_sel_hi:[1,0,0]\n\t"
        "v_fma_mix_f32 %5, %16, %20, %24 op_sel_hi:[1,0,0]\n\t"
        "v_fma_mix_f32 %6, %13, %21, %25 op_sel_hi:[1,0,0]\n\t"
        "v_fma_mix_f32 %7, %17, %21, %25 op_sel_hi:[1,0,0]\n\t"
        "v_fma_mix_f32 %8, %14, %22, %26 op_sel_hi:[1,0,0]\n\t"
        "v_fma_mix_f32 %9, %18, %22, %26 op_sel_hi:[1,0,0]\n\t"
        "v_fma_mix_f32 %10, %15, %23, %27 op_sel_hi:[1,0,0]\n\t"
        "v_fma_mix_f32 %11, %19, %23, %27 op_sel_hi:[1,0,0]\n\t"
        "v_cvt_pk_bf16_f32 %0, %4, %5\n\t"
        "v_cvt_pk_bf16_f32 %1, %6, %7\n\t"
        "v_cvt_pk_bf16_f32 %2, %8, %9\n\t"
        "v_cvt_pk_bf16_f32 %3, %10, %11"
        : "=&v"(o[0][kk]), "=&v"(o[1][kk]), "=&v"(o[2][kk]), "=&v"(o[3][kk]),
          "=&v"(t0), "=&v"(t1), "=&v"(t2), "=&v"(t3), "=&v"(t4), "=&v"(t5), "=&v"(t6), "=&v"(t7)
        : "v"(qa[0]), "v"(qa[1]), "v"(qa[2]), "v"(qa[3]), "v"(qb[0]), "v"(qb[1]), "v"(qb[2]), "v"(qb[3]),
          "v"(S[0]), "v"(S[1]), "v"(S[2]), "v"(S[3]), "v"(C[0]), "v"(C[1]), "v"(C[2]), "v"(C[3]));
}
DI u32x4 dequant_col(const u32x4 (&q)[8], int j, float S, float C) {
    u32x4 o; float t0, t1, t2, t3, t4, t5, t6, t7;
    asm volatile(
        "v_fma_mix_f32 %4, %12, %20, %21 op_sel_hi:[1,0,0]\n\t"
        "v_fma_mix_f32 %5, %13, %20, %21 op_sel_hi:[1,0,0]\n\t"
        "v_fma_mix_f32 %6, %14, %20, %21 op_sel_hi:[1,0,0]\n\t"
        "v_fma_mix_f32 %7, %15, %20, %21 op_sel_hi:[1,0,0]\n\t"
        "v_fma_mix_f32 %8, %16, %20, %21 op_sel_hi:[1,0,0]\n\t"
        "v_fma_mix_f32 %9, %17, %20, %21 op_sel_hi:[1,0,0]\n\t"
        "v_fma_mix_f32 %10, %18, %20, %21 op_sel_hi:[1,0,0]\n\t"
        "v_fma_mix_f32 %11, %19, %20, %21 op_sel_hi:[1,0,0]\n\t"
        "v_cvt_pk_bf16_f32 %0, %4, %5\n\t"
        "v_cvt_pk_bf16_f32 %1, %6, %7\n\t"
        "v_cvt_pk_bf16_f32 %2, %8, %9\n\t"
        "v_cvt_pk_bf16_f32 %3, %10, %11"
        : "=&v"(o[0]), "=&v"(o[1]), "=&v"(o[2]), "=&v"(o[3]),
          "=&v"(t0), "=&v"(t1), "=&v"(t2), "=&v"(t3), "=&v"(t4), "=&v"(t5), "=&v"(t6), "=&v"(t7)
        : "v"(q[0][j]), "v"(q[1][j]), "v"(q[2][j]), "v"(q[3][j]), "v"(q[4][j]), "v"(q[5][j]), "v"(q[6][j]), "v"(q[7][j]), "v"(S), "v"(C));
    return o;
}
DI void make_scales(float (&S)[4], float (&C)[4], const u32x2& s2, const u32x2& z2) {
#pragma unroll
    for (int j = 0; j < 4; ++j) {
        const float s = __uint_as_float((j & 1) ? (s2[j >> 1] & 0xffff0000u) : (s2[j >> 1] << 16));
        const float z = __uint_as_float((j & 1) ? (z2[j >> 1] & 0xffff0000u) : (z2[j >> 1] << 16));
        S[j] = fmul(s, 16777216.0f);
        C[j] = fmul(-z, s);
    }
}
struct PCtx { unsigned vq[8], vx[8], m0a, wa0, wa1; __amdgpu_buffer_rsrc_t srdQ, srdX; };
template <int J> DI void ld_x(const PCtx& c, unsigned sx) { dma16o<(J & 3) * 1024>(c.vx[J], c.srdX, sx); }
template <bool ISSUE, bool NEWS>
DI void pstep(u32x4 (&q)[8], u32x4 (&qn)[8], float (&S)[4], float (&C)[4], u32x2& s2, u32x2& z2, const PCtx& c, int bufoff, int ns) {
    u32x4 o[4];
    const unsigned sq = (unsigned)ns * (BK * QROW), sx = (unsigned)ns * (BK * 2), mb = c.m0a + (unsigned)(ns & 3) * A_STAGE;
    if constexpr (ISSUE) { set_m0(mb); ld_q(qn[0], c.vq[0], c.srdQ, sq); ld_q(qn[1], c.vq[1], c.srdQ, sq); }
    wait_q<ISSUE ? 10 : 8>(q);
#pragma unroll
    for (int kk = 0; kk < 4; ++kk) {
        dequant_pair(o, kk, q[2 * kk], q[2 * kk + 1], S, C);
        if constexpr (ISSUE) { if (kk < 3) { ld_q(qn[2 * kk + 2], c.vq[2 * kk + 2], c.srdQ, sq); ld_q(qn[2 * kk + 3], c.vq[2 * kk + 3], c.srdQ, sq); } }
    }
    if constexpr (ISSUE && SEQ == 2) { ld_x<0>(c, sx); ld_x<1>(c, sx); }
#pragma unroll
    for (int j = 0; j < 4; ++j) *(LAS u32x4*)(uintptr_t)(((j >> 1) ? c.wa1 : c.wa0) + (unsigned)(j * 128 + bufoff)) = o[j];
    if constexpr (ISSUE) { if (SEQ != 2) { ld_x<0>(c, sx); ld_x<1>(c, sx); } ld_x<2>(c, sx); ld_x<3>(c, sx); set_m0(mb + 4096u); }
    if constexpr (NEWS) {
        asm volatile("" : "+v"(s2), "+v"(z2) :: "memory");
        make_scales(S, C, s2, z2);
    } else asm volatile("s_nop 0");
    if constexpr (ISSUE) { ld_x<4>(c, sx); ld_x<5>(c, sx); ld_x<6>(c, sx); ld_x<7>(c, sx); }
    if constexpr (ISSUE) WAIT_V(16); else WAIT_V(0);
}

struct Frag { bf16x8 x[4]; bf16x8 w[8]; };
DI void read_frags(Frag& f, unsigned xa, unsigned w0, unsigned w1, int bufoff) {
    f.x[0] = *(LAS const bf16x8*)(uintptr_t)(xa);
#pragma unroll
    for (int t = 0; t < 8; ++t)
        f.w[t] = *(LAS const bf16x8*)(uintptr_t)(((t & 1) ? w1 : w0) + (unsigned)(t * 2048 + bufoff));
#pragma unroll
    for (int m = 1; m < 4; ++m)
        f.x[m] = *(LAS const bf16x8*)(uintptr_t)(xa + (unsigned)(m * 2048));
}
DI void mma(f32x4 (&acc)[4][8], const Frag& f) {
#pragma unroll
    for (int m = 0; m < 4; ++m)
#pragma unroll
        for (int t = 0; t < 8; ++t)
            acc[m][t] = __builtin_amdgcn_mfma_f32_16x16x32_bf16(f.w[t], f.x[m], acc[m][t], 0, 0, 0);
}
DI void interleave() {
#pragma unroll
    for (int i = 0; i < 12; ++i) {
        __builtin_amdgcn_sched_group_barrier(0x008, 2, 0);
        __builtin_amdgcn_sched_group_barrier(0x100, 1, 0);
    }
    __builtin_amdgcn_sched_group_barrier(0x008, 8, 0);
}

__global__ __launch_bounds__(512, 2)
void w4a16_kernel(const unsigned short* __restrict__ x, const int* __restrict__ qw, const unsigned short* __restrict__ sc,
                  const unsigned short* __restrict__ zr, unsigned short* __restrict__ out, char* __restrict__ ws) {
    __shared__ __attribute__((aligned(1024))) char smem[LDS_TOTAL];
    const unsigned lbase = (unsigned)(uintptr_t)(LAS char*)smem;
    const int lane = threadIdx.x & 63;
    const int wid = __builtin_amdgcn_readfirstlane(threadIdx.x >> 6);
    const int bid = blockIdx.x, xcd = bid & 7, idx = bid >> 3;
    const int tn0 = (xcd >> 1) * 7 + (xcd & 1) * 3;
    const __amdgpu_buffer_rsrc_t srdQ = __builtin_amdgcn_make_buffer_rsrc((void*)qw, (short)0, K_ * N_ * 4, 0x00020000);
    STAMP_DECL;
    STAMP(0);
    unsigned long long dg0, dg2, dg19, dg21, dc0, dc2, dc19, dc21;
#define RT(v) asm volatile("s_memrealtime %0\n\ts_waitcnt lgkmcnt(0)" : "=s"(v) :: "memory")
#define CT(v) asm volatile("s_memtime %0\n\ts_waitcnt lgkmcnt(0)" : "=s"(v) :: "memory")
    RT(dg0); CT(dc0);
    if (idx >= 28) {
        if (HELP && wid == 0) {
            const __amdgpu_buffer_rsrc_t srdXh = __builtin_amdgcn_make_buffer_rsrc((void*)x, (short)0, M_ * K_ * 2, 0x00020000);
            const int h = idx - 28;
            const unsigned v = (unsigned)((h * 16 + (lane >> 4)) * QROW + tn0 * 512 + (lane & 15) * 128);
            const unsigned vxh = (unsigned)((xcd * 256 + h * 64 + lane) * XROW);
            if (HELPS) {
                const __amdgpu_buffer_rsrc_t srdSh = __builtin_amdgcn_make_buffer_rsrc((void*)sc, (short)0, 8 * N_ * 2, 0x00020000);
                const __amdgpu_buffer_rsrc_t srdZh = __builtin_amdgcn_make_buffer_rsrc((void*)zr, (short)0, 8 * N_ * 2, 0x00020000);
                const unsigned vsh = lane < 16 ? (unsigned)((2 * h + (lane >> 3)) * N_ * 2 + tn0 * 256 + (lane & 7) * 128) : 0x80000000u;
                dma4(lbase, vsh, srdSh, 0u); dma4(lbase, vsh, srdZh, 0u);
            }
            for (int s = HSTART; s < NIT; ++s) {
                if (HELPX) dma4(lbase, vxh, srdXh, (unsigned)(s * 128));
#pragma unroll
                for (int u = 0; u < 4; ++u) dma4(lbase, v, srdQ, (unsigned)(s * BK + u * 4) * QROW);
            }
            WAIT_V(0);
            if (DIAG == 6) { RT(dg21); unsigned long long now; do { __builtin_amdgcn_s_sleep(4); RT(now); } while (now - dg0 < 3000 + 8 * (dg21 - dg0)); }
        }
        return;
    }
    int tm, tn;
    if (idx < 24) { tm = idx & 7; tn = tn0 + (xcd & 1) + (idx >> 3); }
    else { tm = (xcd & 1) * 4 + (idx - 24); tn = (xcd >> 1) * 7 + 3; }
    const int m0 = tm * BM, n0 = tn * BN;

    if (wid >= 4) {
        const int pw = wid - 4;
        PCtx c;
        c.srdQ = srdQ;
        c.srdX = __builtin_amdgcn_make_buffer_rsrc((void*)((const char*)x - 4096), (short)0, M_ * K_ * 2 + 4096, 0x00020000);
        const __amdgpu_buffer_rsrc_t srdS = __builtin_amdgcn_make_buffer_rsrc((void*)sc, (short)0, 8 * N_ * 2, 0x00020000);
        const __amdgpu_buffer_rsrc_t srdZ = __builtin_amdgcn_make_buffer_rsrc((void*)zr, (short)0, 8 * N_ * 2, 0x00020000);
        const int kb = 2 * pw + (lane >> 5), nb = lane & 31;
#pragma unroll
        for (int i = 0; i < 8; ++i) c.vq[i] = (unsigned)((kb * 8 + i) * QROW + (n0 + 4 * nb) * 4);
        const unsigned vs = (unsigned)((n0 + 4 * nb) * 2);
        u32x4 q0[8], q1[8]; u32x2 s2, z2;
        ld8(s2, vs, srdS, 0u); ld8(z2, vs, srdZ, 0u);
#pragma unroll
        for (int i = 0; i < 8; ++i) ld_q(q0[i], c.vq[i], c.srdQ, 0u);
#pragma unroll
        for (int j = 0; j < 8; ++j) {
            const int row = pw * 64 + j * 8 + (lane >> 3);
            c.vx[j] = (unsigned)((m0 + row) * XROW + (((lane & 7) ^ ((row >> 1) & 7)) * 16) + 4096 - (j & 3) * 1024);
        }
        c.m0a = lbase + LDS_A + pw * 8192;
        set_m0(c.m0a);
        asm volatile("s_nop 0");
        ld_x<0>(c, 0u); ld_x<1>(c, 0u); ld_x<2>(c, 0u); ld_x<3>(c, 0u);
        set_m0(c.m0a + 4096u);
        asm volatile("s_nop 0");
        ld_x<4>(c, 0u); ld_x<5>(c, 0u); ld_x<6>(c, 0u); ld_x<7>(c, 0u);
        const int rb = 32 * (nb >> 3) + 16 * (nb & 1) + 4 * ((nb >> 1) & 3);
        const int f = ((rb >> 1) & 7) ^ ((rb >> 4) & 1);
        c.wa0 = lbase + (unsigned)(LDS_B + rb * 128 + ((kb ^ f) * 16));
        c.wa1 = c.wa0 ^ 16u;
        STAMP(1);
        float S[4], C[4];
        asm volatile("s_waitcnt vmcnt(16)" : "+v"(s2), "+v"(z2) :: "memory");
        make_scales(S, C, s2, z2);
        for (int b = 0; b < NIT - 2; b += 2) {
            ld8(s2, vs, srdS, (unsigned)((b / 2 + 1) * N_ * 2)); ld8(z2, vs, srdZ, (unsigned)((b / 2 + 1) * N_ * 2));
            pstep<true, false>(q0, q1, S, C, s2, z2, c, 0, b + 1);
            WAIT_L0();
            BAR();
            PSTAMP(2 + b);
            pstep<true, true>(q1, q0, S, C, s2, z2, c, B_STAGE, b + 2);
            WAIT_L0();
            BAR();
            PSTAMP(3 + b);
        }
        pstep<true, false>(q0, q1, S, C, s2, z2, c, 0, NIT - 1);
        WAIT_L0();
        BAR();
        PSTAMP(16);
        pstep<false, false>(q1, q0, S, C, s2, z2, c, B_STAGE, 0);
        WAIT_L0();
        BAR();
        STAMP(17);
        BAR();
    } else {
        const int w = wid;
        {
            constexpr int NX0 = CX0 ? 2048 : 0, NQ = CQ * 1024, NXG = (CQ - CX0) * 256, TOT = NX0 + NQ + NXG, LPW = (TOT + 111) / 112;
            static_assert(LPW <= 64, "one load per wave");
            const int l = lane * 112 + idx * 4 + w;
            if (TOT > 0 && l < TOT) {
                int lq = l - NX0;
                const char* p;
                if (lq < 0) p = (const char*)x + (size_t)l * XROW;
                else if (lq < NQ) p = (const char*)qw + (size_t)(lq >> 4) * QROW + tn0 * 512 + (lq & 15) * 128;
                else { const int lg = lq - NQ; p = (const char*)x + (size_t)(xcd * 256 + (lg & 255)) * XROW + (CX0 + (lg >> 8)) * 128; }
                const unsigned v = *(const volatile unsigned*)p;
                asm volatile("" :: "v"(v));
            }
        }
        const int fr = lane & 15, fq = lane >> 4;
        const unsigned sw = (unsigned)((fq ^ (fr >> 1)) * 16);
        unsigned xa0 = lbase + (unsigned)(LDS_A + (w * 64 + fr) * 128) + sw;
        unsigned w00 = lbase + (unsigned)(LDS_B + fr * 128) + sw;
        unsigned w01 = w00 ^ 16u, w10 = w00 ^ 64u, w11 = w00 ^ 80u;
        asm volatile("" : "+v"(w00), "+v"(w01), "+v"(w10), "+v"(w11));
        f32x4 acc[4][8];
#pragma unroll
        for (int m = 0; m < 4; ++m)
#pragma unroll
            for (int t = 0; t < 8; ++t) { acc[m][t] = (f32x4){0.f, 0.f, 0.f, 0.f}; asm volatile("" : "+v"(acc[m][t])); }
        Frag f0, f1;
        asm volatile("" :: "s"(out));
        BAR();
        RT(dg2); CT(dc2);
        read_frags(f0, xa0, w00, w01, 0);
        for (int it = 0; it < NIT; it += 2) {
            unsigned xe = xa0 + (unsigned)((it & 2) * A_STAGE);
            unsigned xn = xa0 + (unsigned)(((it + 2) & 2) * A_STAGE);
            asm volatile("" : "+v"(xe), "+v"(xn));
            SB0();
            read_frags(f1, xe ^ 64u, w10, w11, 0);
            mma(acc, f0);
            interleave();
            SB0();
            WAIT_L0();
            BAR();
            LSTAMP(3 + it);
            SB0();
            read_frags(f0, xe + A_STAGE, w00, w01, B_STAGE);
            mma(acc, f1);
            interleave();
            SB0();
            read_frags(f1, (xe ^ 64u) + A_STAGE, w10, w11, B_STAGE);
            mma(acc, f0);
            interleave();
            SB0();
            WAIT_L0();
            BAR();
            LSTAMP(4 + it);
            SB0();
            read_frags(f0, xn, w00, w01, 0);
            mma(acc, f1);
            interleave();
            SB0();
        }
        SB0(); RT(dg19); CT(dc19); SB0();
        const unsigned eb = lbase + (unsigned)(A_STAGE + w * (64 * OROW));
        const unsigned ew = eb + (unsigned)(fr * OROW + fq * 16);
        const unsigned er = eb + (unsigned)(fq * OROW + fr * 16);
        const __amdgpu_buffer_rsrc_t srdO = __builtin_amdgcn_make_buffer_rsrc((void*)out, (short)0, M_ * N_ * 2, 0x00020000);
        const int ovo = fq * (N_ * 2) + fr * 16;
        const int oso = ((m0 + w * 64) * N_ + n0) * 2;
#pragma unroll
        for (int m = 0; m < 4; ++m) {
#pragma unroll
            for (int pp = 0; pp < 4; ++pp) {
                u32x4 v;
                v[0] = cvt_pk_bf16(acc[m][2 * pp][0], acc[m][2 * pp][1]);
                v[1] = cvt_pk_bf16(acc[m][2 * pp][2], acc[m][2 * pp][3]);
                v[2] = cvt_pk_bf16(acc[m][2 * pp + 1][0], acc[m][2 * pp + 1][1]);
                v[3] = cvt_pk_bf16(acc[m][2 * pp + 1][2], acc[m][2 * pp + 1][3]);
                *(LAS u32x4*)(uintptr_t)(ew + (unsigned)(m * 16 * OROW + pp * 64)) = v;
            }
#pragma unroll
            for (int i = 0; i < 4; ++i) {
                const u32x4 v = *(LAS const u32x4*)(uintptr_t)(er + (unsigned)((m * 16 + i * 4) * OROW));
                __builtin_amdgcn_raw_buffer_store_b128(v, srdO, ovo, oso + (m * 16 + i * 4) * (N_ * 2), STAUX);
            }
        }
        STAMP(20);
        WAIT_V(0);
        RT(dg21); CT(dc21);
        const unsigned rp = (unsigned)(dc2 - dc0) * 40u / (unsigned)(dg2 - dg0), rl = (unsigned)(dc19 - dc2) * 40u / (unsigned)(dg19 - dg2), re = (unsigned)(dc21 - dc19) * 40u / (unsigned)(dg21 - dg19);
        { const unsigned long long q = DIAG == 10 ? rp : DIAG == 11 ? 1200 - rp : DIAG == 12 ? rl : DIAG == 13 ? 1200 - rl : DIAG == 14 ? re : DIAG == 1 ? dg2 - dg0 : DIAG == 2 ? dg19 - dg2 : DIAG == 3 ? dg21 - dg19 : DIAG == 4 ? (dc19 - dc2) / 24 : DIAG == 5 ? dg21 - dg0 : 0;
          unsigned long long now; do { __builtin_amdgcn_s_sleep(4); RT(now); } while (now - dg0 < 3000 + 8 * q); }
    }
    STAMP_OUT();
}
}

extern "C" void kernel_launch(void* const* d_in, const int* in_sizes, int n_in,
                              void* d_out, int out_size, void* d_ws, size_t ws_size,
                              hipStream_t stream) {
    w4a16_kernel<<<256, 512, 0, stream>>>((const unsigned short*)d_in[0], (const int*)d_in[1],
        (const unsigned short*)d_in[2], (const unsigned short*)d_in[3], (unsigned short*)d_out, (char*)d_ws);
}
```

```cpp
#include <hip/hip_runtime.h>
#include <hip/hip_bf16.h>

typedef short bf16x8 __attribute__((ext_vector_type(8)));
typedef float f32x2 __attribute__((ext_vector_type(2)));
typedef float f32x4 __attribute__((ext_vector_type(4)));
typedef float f32x16 __attribute__((ext_vector_type(16)));
typedef __bf16 bf16x2 __attribute__((ext_vector_type(2)));
typedef unsigned u32x4 __attribute__((ext_vector_type(4)));
typedef unsigned u32x2 __attribute__((ext_vector_type(2)));
#define LAS __attribute__((address_space(3)))
#define DI __device__ __forceinline__

#define CQ 1
#define STAUX 2
#define M32 0

namespace {
constexpr int M_ = 2048, N_ = 3584, K_ = 1024;
constexpr int BM = 256, BN = 128, BK = 64, NIT = K_ / BK;
constexpr int A_STAGE = BM * BK * 2;
constexpr int B_STAGE = BN * BK * 2;
constexpr int LDS_A = 0, LDS_B = 4 * A_STAGE;
constexpr int LDS_TOTAL = LDS_B + 2 * B_STAGE;
constexpr int QROW = N_ * 4;
constexpr int XROW = K_ * 2;
constexpr int OROW = 272;

#define STAMP(i)
#define STAMP_DECL
#define STAMP_OUT()
#define WAIT_V(n) asm volatile("s_waitcnt vmcnt(%0)" ::"n"(n) : "memory")
#define WAIT_L0() asm volatile("s_waitcnt lgkmcnt(0)" ::: "memory")
#define BAR() do { asm volatile("" ::: "memory"); __builtin_amdgcn_s_barrier(); asm volatile("" ::: "memory"); } while (0)
#define SB0() __builtin_amdgcn_sched_barrier(0)

DI unsigned pack_bf16(float lo, float hi) { return __builtin_bit_cast(unsigned, __builtin_convertvector((f32x2){lo, hi}, bf16x2)); }
DI float fmul(float a, float b) { float r; asm volatile("v_mul_f32 %0, %1, %2" : "=v"(r) : "v"(a), "v"(b)); return r; }
DI void ld_q(u32x4& q, unsigned vq, __amdgpu_buffer_rsrc_t srd, unsigned so) {
    asm volatile("buffer_load_dwordx4 %0, %1, %2, %3 offen" : "=v"(q) : "v"(vq), "s"(srd), "s"(so) : "memory");
}
DI void ld8(u32x2& q, unsigned v, __amdgpu_buffer_rsrc_t srd, unsigned so) {
    asm volatile("buffer_load_dwordx2 %0, %1, %2, %3 offen" : "=v"(q) : "v"(v), "s"(srd), "s"(so) : "memory");
}
DI void set_m0(unsigned m0v) { asm volatile("s_mov_b32 m0, %0\n\ts_nop 0" :: "s"(m0v) : "memory"); }
template <int OFF> DI void dma16(unsigned v, __amdgpu_buffer_rsrc_t srd, unsigned so) {
    asm volatile("buffer_load_dwordx4 %0, %1, %2 offen offset:%3 lds" :: "v"(v), "s"(srd), "s"(so), "n"(OFF) : "memory");
}
DI void touch(unsigned v, __amdgpu_buffer_rsrc_t srd, unsigned so) {
    asm volatile("buffer_load_dword %0, %1, %2 offen lds" :: "v"(v), "s"(srd), "s"(so) : "memory");
}
template <int W> DI void wait_q(u32x4 (&q)[8]) {
    asm volatile("s_waitcnt vmcnt(%8)"
                 : "+v"(q[0]), "+v"(q[1]), "+v"(q[2]), "+v"(q[3]), "+v"(q[4]), "+v"(q[5]), "+v"(q[6]), "+v"(q[7]) : "n"(W) : "memory");
}
DI void dequant_pair(u32x4 (&o)[4], int kk, const u32x4& qa, const u32x4& qb, const float (&S)[4], const float (&C)[4]) {
    float t0, t1, t2, t3, t4, t5, t6, t7;
    asm volatile(
        "v_fma_mix_f32 %4, %12, %20, %24 op_sel_hi:[1,0,0]\n\t"
        "v_fma_mix_f32 %5, %16, %20, %24 op_sel_hi:[1,0,0]\n\t"
        "v_fma_mix_f32 %6, %13, %21, %25 op_sel_hi:[1,0,0]\n\t"
        "v_fma_mix_f32 %7, %17, %21, %25 op_sel_hi:[1,0,0]\n\t"
        "v_fma_mix_f32 %8, %14, %22, %26 op_sel_hi:[1,0,0]\n\t"
        "v_fma_mix_f32 %9, %18, %22, %26 op_sel_hi:[1,0,0]\n\t"
        "v_fma_mix_f32 %10, %15, %23, %27 op_sel_hi:[1,0,0]\n\t"
        "v_fma_mix_f32 %11, %19, %23, %27 op_sel_hi:[1,0,0]\n\t"
        "v_cvt_pk_bf16_f32 %0, %4, %5\n\t"
        "v_cvt_pk_bf16_f32 %1, %6, %7\n\t"
        "v_cvt_pk_bf16_f32 %2, %8, %9\n\t"
        "v_cvt_pk_bf16_f32 %3, %10, %11"
        : "=&v"(o[0][kk]), "=&v"(o[1][kk]), "=&v"(o[2][kk]), "=&v"(o[3][kk]),
          "=&v"(t0), "=&v"(t1), "=&v"(t2), "=&v"(t3), "=&v"(t4), "=&v"(t5), "=&v"(t6), "=&v"(t7)
        : "v"(qa[0]), "v"(qa[1]), "v"(qa[2]), "v"(qa[3]), "v"(qb[0]), "v"(qb[1]), "v"(qb[2]), "v"(qb[3]),
          "v"(S[0]), "v"(S[1]), "v"(S[2]), "v"(S[3]), "v"(C[0]), "v"(C[1]), "v"(C[2]), "v"(C[3]));
}
DI void make_scales(float (&S)[4], float (&C)[4], const u32x2& s2, const u32x2& z2) {
#pragma unroll
    for (int j = 0; j < 4; ++j) {
        const float s = __uint_as_float((j & 1) ? (s2[j >> 1] & 0xffff0000u) : (s2[j >> 1] << 16));
        const float z = __uint_as_float((j & 1) ? (z2[j >> 1] & 0xffff0000u) : (z2[j >> 1] << 16));
        S[j] = fmul(s, 16777216.0f);
        C[j] = fmul(-z, s);
    }
}
struct PCtx { unsigned vq[8], vx[8], m0a, wa0, wa1; __amdgpu_buffer_rsrc_t srdQ, srdX; };
template <int J> DI void ld_x(const PCtx& c, unsigned sx) { dma16<(J & 3) * 1024>(c.vx[J], c.srdX, sx); }
template <bool ISSUE, bool NEWS>
DI void pstep(u32x4 (&q)[8], u32x4 (&qn)[8], float (&S)[4], float (&C)[4], u32x2& s2, u32x2& z2, const PCtx& c, int bufoff, int ns) {
    u32x4 o[4];
    const unsigned sq = (unsigned)ns * (BK * QROW), sx = (unsigned)ns * (BK * 2), mb = c.m0a + (unsigned)(ns & 3) * A_STAGE;
    if constexpr (ISSUE) { set_m0(mb); ld_q(qn[0], c.vq[0], c.srdQ, sq); ld_q(qn[1], c.vq[1], c.srdQ, sq); }
    wait_q<ISSUE ? 10 : 8>(q);
#pragma unroll
    for (int kk = 0; kk < 4; ++kk) {
        dequant_pair(o, kk, q[2 * kk], q[2 * kk + 1], S, C);
        if constexpr (ISSUE) { if (kk < 3) { ld_q(qn[2 * kk + 2], c.vq[2 * kk + 2], c.srdQ, sq); ld_q(qn[2 * kk + 3], c.vq[2 * kk + 3], c.srdQ, sq); } }
    }
    if constexpr (ISSUE) { ld_x<0>(c, sx); ld_x<1>(c, sx); }
#pragma unroll
    for (int j = 0; j < 4; ++j) *(LAS u32x4*)(uintptr_t)(((j >> 1) ? c.wa1 : c.wa0) + (unsigned)(j * 128 + bufoff)) = o[j];
    if constexpr (ISSUE) { ld_x<2>(c, sx); ld_x<3>(c, sx); set_m0(mb + 4096u); }
    if constexpr (NEWS) {
        asm volatile("" : "+v"(s2), "+v"(z2) :: "memory");
        make_scales(S, C, s2, z2);
    }
    if constexpr (ISSUE) { ld_x<4>(c, sx); ld_x<5>(c, sx); ld_x<6>(c, sx); ld_x<7>(c, sx); WAIT_V(16); } else WAIT_V(0);
    WAIT_L0();
    BAR();
}

struct Frag { bf16x8 x[4]; bf16x8 w[8]; };
typedef f32x4 Acc[4][8];
DI void read_frags(Frag& f, unsigned xa, unsigned, unsigned w0, unsigned w1, int bufoff) {
    f.x[0] = *(LAS const bf16x8*)(uintptr_t)(xa);
#pragma unroll
    for (int t = 0; t < 8; ++t)
        f.w[t] = *(LAS const bf16x8*)(uintptr_t)(((t & 1) ? w1 : w0) + (unsigned)(t * 2048 + bufoff));
#pragma unroll
    for (int m = 1; m < 4; ++m)
        f.x[m] = *(LAS const bf16x8*)(uintptr_t)(xa + (unsigned)(m * 2048));
}
DI void mma(Acc& acc, const Frag& f) {
#pragma unroll
    for (int m = 0; m < 4; ++m)
#pragma unroll
        for (int tt = 0; tt < 8; ++tt) {
            const int t = (m & 1) ? 7 - tt : tt;
            acc[m][t] = __builtin_amdgcn_mfma_f32_16x16x32_bf16(f.w[t], f.x[m], acc[m][t], 0, 0, 0);
        }
}
DI void interleave() {
#pragma unroll
    for (int i = 0; i < 12; ++i) {
        __builtin_amdgcn_sched_group_barrier(0x008, 2, 0);
        __builtin_amdgcn_sched_group_barrier(0x100, 1, 0);
    }
    __builtin_amdgcn_sched_group_barrier(0x008, 8, 0);
}

__global__ __launch_bounds__(512, 2)
void w4a16_kernel(const unsigned short* __restrict__ x, const int* __restrict__ qw, const unsigned short* __restrict__ sc,
                  const unsigned short* __restrict__ zr, unsigned short* __restrict__ out, char* __restrict__ ws) {
    __shared__ __attribute__((aligned(1024))) char smem[LDS_TOTAL];
    STAMP_DECL;
    STAMP(0);
    const unsigned lbase = (unsigned)(uintptr_t)(LAS char*)smem;
    const int lane = threadIdx.x & 63;
    const int wid = __builtin_amdgcn_readfirstlane(threadIdx.x >> 6);
    const int bid = blockIdx.x, xcd = bid & 7, idx = bid >> 3;
    const int tn0 = (xcd >> 1) * 7 + (xcd & 1) * 3;
    const __amdgpu_buffer_rsrc_t srdQ = __builtin_amdgcn_make_buffer_rsrc((void*)qw, (short)0, K_ * N_ * 4, 0x00020000);
    const __amdgpu_buffer_rsrc_t srdS = __builtin_amdgcn_make_buffer_rsrc((void*)sc, (short)0, 8 * N_ * 2, 0x00020000);
    const __amdgpu_buffer_rsrc_t srdZ = __builtin_amdgcn_make_buffer_rsrc((void*)zr, (short)0, 8 * N_ * 2, 0x00020000);
    if (idx >= 28) {
        if (wid == 0) {
            const __amdgpu_buffer_rsrc_t srdXh = __builtin_amdgcn_make_buffer_rsrc((void*)x, (short)0, M_ * K_ * 2, 0x00020000);
            const int h = idx - 28;
            const unsigned vq = (unsigned)((h * 16 + (lane >> 4)) * QROW + tn0 * 512 + (lane & 15) * 128);
            const unsigned vx = (unsigned)((xcd * 256 + h * 64 + lane) * XROW);
            const unsigned vs = lane < 16 ? (unsigned)((2 * h + (lane >> 3)) * N_ * 2 + tn0 * 256 + (lane & 7) * 128) : 0x80000000u;
            set_m0(lbase);
            touch(vs, srdS, 0u); touch(vs, srdZ, 0u);
            for (int s = CQ; s < NIT; ++s) {
                touch(vx, srdXh, (unsigned)(s * 128));
#pragma unroll
                for (int u = 0; u < 4; ++u) touch(vq, srdQ, (unsigned)(s * BK + u * 4) * QROW);
            }
            WAIT_V(0);
        }
        STAMP(21);
        STAMP_OUT();
        return;
    }
    int tm, tn;
    if (idx < 24) { tm = idx & 7; tn = tn0 + (xcd & 1) + (idx >> 3); }
    else { tm = (xcd & 1) * 4 + (idx - 24); tn = (xcd >> 1) * 7 + 3; }
    const int m0 = tm * BM, n0 = tn * BN;

    if (wid >= 4) {
        const int pw = wid - 4;
        PCtx c;
        c.srdQ = srdQ;
        c.srdX = __builtin_amdgcn_make_buffer_rsrc((void*)((const char*)x - 4096), (short)0, M_ * K_ * 2 + 4096, 0x00020000);
        const int kb = 2 * pw + (lane >> 5), nb = lane & 31;
#pragma unroll
        for (int i = 0; i < 8; ++i) c.vq[i] = (unsigned)((kb * 8 + i) * QROW + (n0 + 4 * nb) * 4);
        const unsigned vs = (unsigned)((n0 + 4 * nb) * 2);
        u32x4 q0[8], q1[8]; u32x2 s2, z2;
        ld8(s2, vs, srdS, 0u); ld8(z2, vs, srdZ, 0u);
#pragma unroll
        for (int i = 0; i < 8; ++i) ld_q(q0[i], c.vq[i], c.srdQ, 0u);
#pragma unroll
        for (int j = 0; j < 8; ++j) {
            const int row = pw * 64 + j * 8 + (lane >> 3);
            c.vx[j] = (unsigned)((m0 + row) * XROW + (((lane & 7) ^ ((row >> 1) & 7)) * 16) + 4096 - (j & 3) * 1024);
        }
        c.m0a = lbase + LDS_A + pw * 8192;
        set_m0(c.m0a);
        ld_x<0>(c, 0u); ld_x<1>(c, 0u); ld_x<2>(c, 0u); ld_x<3>(c, 0u);
        set_m0(c.m0a + 4096u);
        ld_x<4>(c, 0u); ld_x<5>(c, 0u); ld_x<6>(c, 0u); ld_x<7>(c, 0u);
        const int rb = 32 * (nb >> 3) + 16 * (nb & 1) + 4 * ((nb >> 1) & 3);
        const int f = ((rb >> 1) & 7) ^ ((rb >> 4) & 1);
        c.wa0 = lbase + (unsigned)(LDS_B + rb * 128 + ((kb ^ f) * 16));
        c.wa1 = c.wa0 ^ 16u;
        STAMP(1);
        float S[4], C[4];
        asm volatile("s_waitcnt vmcnt(16)" : "+v"(s2), "+v"(z2) :: "memory");
        make_scales(S, C, s2, z2);
        for (int b = 0; b < NIT - 2; b += 2) {
            ld8(s2, vs, srdS, (unsigned)((b / 2 + 1) * N_ * 2)); ld8(z2, vs, srdZ, (unsigned)((b / 2 + 1) * N_ * 2));
            pstep<true, false>(q0, q1, S, C, s2, z2, c, 0, b + 1);
            pstep<true, true>(q1, q0, S, C, s2, z2, c, B_STAGE, b + 2);
        }
        pstep<true, false>(q0, q1, S, C, s2, z2, c, 0, NIT - 1);
        pstep<false, false>(q1, q0, S, C, s2, z2, c, B_STAGE, 0);
        STAMP(17);
        BAR();
    } else {
        const int w = wid;
        {
            constexpr int NX0 = 2048, NQ = CQ * 1024, TOT = NX0 + NQ + (CQ - 1) * 256;
            static_assert(TOT <= 64 * 112, "one load per wave");
            const int l = lane * 112 + idx * 4 + w;
            if (l < TOT) {
                const int lq = l - NX0, lg = lq - NQ;
                const char* p = lq < 0 ? (const char*)x + (size_t)l * XROW
                              : lg < 0 ? (const char*)qw + (size_t)(lq >> 4) * QROW + tn0 * 512 + (lq & 15) * 128
                                       : (const char*)x + (size_t)(xcd * 256 + (lg & 255)) * XROW + (1 + (lg >> 8)) * 128;
                const unsigned v = *(const volatile unsigned*)p;
                asm volatile("" :: "v"(v));
            }
        }
        const int fr = lane & 15, fq = lane >> 4;
        const unsigned sw = (unsigned)((fq ^ (fr >> 1)) * 16);
        const unsigned xa0 = lbase + (unsigned)(LDS_A + (w * 64 + fr) * 128) + sw;
        unsigned w00 = lbase + (unsigned)(LDS_B + fr * 128) + sw;
        unsigned w01 = w00 ^ 16u, w10 = w00 ^ 64u, w11 = w00 ^ 80u;
#define XQ(a) 0u
        asm volatile("" : "+v"(w00), "+v"(w01), "+v"(w10), "+v"(w11));
        Acc acc;
#pragma unroll
        for (int m = 0; m < (int)(sizeof(acc) / sizeof(acc[0])); ++m)
#pragma unroll
            for (int t = 0; t < (int)(sizeof(acc[0]) / sizeof(acc[0][0])); ++t) { acc[m][t] = 0.f; asm volatile("" : "+v"(acc[m][t])); }
        Frag f0, f1;
        asm volatile("" :: "s"(out));
        BAR();
        STAMP(2);
        read_frags(f0, xa0, XQ(xa0), w00, w01, 0);
        for (int it = 0; it < NIT; it += 2) {
            unsigned xe = xa0 + (unsigned)((it & 2) * A_STAGE);
            unsigned xn = xa0 + (unsigned)(((it + 2) & 2) * A_STAGE);
            asm volatile("" : "+v"(xe), "+v"(xn));
            SB0();
            read_frags(f1, xe ^ 64u, XQ(xe ^ 64u), w10, w11, 0);
            mma(acc, f0);
            interleave();
            SB0();
            WAIT_L0();
            BAR();
            SB0();
            read_frags(f0, xe + A_STAGE, XQ(xe) + A_STAGE, w00, w01, B_STAGE);
            mma(acc, f1);
            interleave();
            SB0();
            read_frags(f1, (xe ^ 64u) + A_STAGE, XQ(xe ^ 64u) + A_STAGE, w10, w11, B_STAGE);
            mma(acc, f0);
            interleave();
            SB0();
            WAIT_L0();
            BAR();
            SB0();
            read_frags(f0, xn, XQ(xn), w00, w01, 0);
            mma(acc, f1);
            interleave();
            SB0();
        }
        STAMP(19);
        const unsigned eb = lbase + (unsigned)(A_STAGE + w * (64 * OROW));
        const unsigned er = eb + (unsigned)((lane >> 4) * OROW + (lane & 15) * 16);
        const __amdgpu_buffer_rsrc_t srdO = __builtin_amdgcn_make_buffer_rsrc((void*)out, (short)0, M_ * N_ * 2, 0x00020000);
        const int ovo = (lane >> 4) * (N_ * 2) + (lane & 15) * 16;
        const int oso = ((m0 + w * 64) * N_ + n0) * 2;
        const unsigned ew = eb + (unsigned)(fr * OROW + fq * 16);
#pragma unroll
        for (int m = 0; m < 4; ++m) {
#pragma unroll
            for (int pp = 0; pp < 4; ++pp) {
                u32x4 v;
                v[0] = pack_bf16(acc[m][2 * pp][0], acc[m][2 * pp][1]);
                v[1] = pack_bf16(acc[m][2 * pp][2], acc[m][2 * pp][3]);
                v[2] = pack_bf16(acc[m][2 * pp + 1][0], acc[m][2 * pp + 1][1]);
                v[3] = pack_bf16(acc[m][2 * pp + 1][2], acc[m][2 * pp + 1][3]);
                *(LAS u32x4*)(uintptr_t)(ew + (unsigned)(m * 16 * OROW + pp * 64)) = v;
            }
#pragma unroll
            for (int i = 0; i < 4; ++i) {
                const u32x4 v = *(LAS const u32x4*)(uintptr_t)(er + (unsigned)((m * 16 + i * 4) * OROW));
                __builtin_amdgcn_raw_buffer_store_b128(v, srdO, ovo, oso + (m * 16 + i * 4) * (N_ * 2), STAUX);
            }
        }
        STAMP(20);
        STAMP(21);
    }
    STAMP_OUT();
}
}

extern "C" void kernel_launch(void* const* d_in, const int* in_sizes, int n_in,
                              void* d_out, int out_size, void* d_ws, size_t ws_size,
                              hipStream_t stream) {
    w4a16_kernel<<<256, 512, 0, stream>>>((const unsigned short*)d_in[0], (const int*)d_in[1],
        (const unsigned short*)d_in[2], (const unsigned short*)d_in[3], (unsigned short*)d_out, (char*)d_ws);
}
```

```cpp
#define TMODE 1
#define TGRID 256
#include <hip/hip_runtime.h>
#include <hip/hip_bf16.h>

typedef short bf16x8 __attribute__((ext_vector_type(8)));
typedef float f32x2 __attribute__((ext_vector_type(2)));
typedef float f32x4 __attribute__((ext_vector_type(4)));
typedef float f32x16 __attribute__((ext_vector_type(16)));
typedef __bf16 bf16x2 __attribute__((ext_vector_type(2)));
typedef unsigned u32x4 __attribute__((ext_vector_type(4)));
typedef unsigned u32x2 __attribute__((ext_vector_type(2)));
#define LAS __attribute__((address_space(3)))
#define DI __device__ __forceinline__

#define CQ 1
#define STAUX 2
#define M32 0

namespace {
constexpr int M_ = 2048, N_ = 3584, K_ = 1024;
constexpr int BM = 256, BN = 128, BK = 64, NIT = K_ / BK;
constexpr int A_STAGE = BM * BK * 2;
constexpr int B_STAGE = BN * BK * 2;
constexpr int LDS_A = 0, LDS_B = 4 * A_STAGE;
constexpr int LDS_TOTAL = LDS_B + 2 * B_STAGE;
constexpr int QROW = N_ * 4;
constexpr int XROW = K_ * 2;
constexpr int OROW = 272;

#define STAMP(i)
#define STAMP_DECL
#define STAMP_OUT()
#define WAIT_V(n) asm volatile("s_waitcnt vmcnt(%0)" ::"n"(n) : "memory")
#define WAIT_L0() asm volatile("s_waitcnt lgkmcnt(0)" ::: "memory")
#define BAR() do { asm volatile("" ::: "memory"); __builtin_amdgcn_s_barrier(); asm volatile("" ::: "memory"); } while (0)
#define SB0() __builtin_amdgcn_sched_barrier(0)

DI unsigned pack_bf16(float lo, float hi) { return __builtin_bit_cast(unsigned, __builtin_convertvector((f32x2){lo, hi}, bf16x2)); }
DI float fmul(float a, float b) { float r; asm volatile("v_mul_f32 %0, %1, %2" : "=v"(r) : "v"(a), "v"(b)); return r; }
DI void ld_q(u32x4& q, unsigned vq, __amdgpu_buffer_rsrc_t srd, unsigned so) {
    asm volatile("buffer_load_dwordx4 %0, %1, %2, %3 offen" : "=v"(q) : "v"(vq), "s"(srd), "s"(so) : "memory");
}
DI void ld8(u32x2& q, unsigned v, __amdgpu_buffer_rsrc_t srd, unsigned so) {
    asm volatile("buffer_load_dwordx2 %0, %1, %2, %3 offen" : "=v"(q) : "v"(v), "s"(srd), "s"(so) : "memory");
}
DI void set_m0(unsigned m0v) { asm volatile("s_mov_b32 m0, %0\n\ts_nop 0" :: "s"(m0v) : "memory"); }
template <int OFF> DI void dma16(unsigned v, __amdgpu_buffer_rsrc_t srd, unsigned so) {
    asm volatile("buffer_load_dwordx4 %0, %1, %2 offen offset:%3 lds" :: "v"(v), "s"(srd), "s"(so), "n"(OFF) : "memory");
}
DI void touch(unsigned v, __amdgpu_buffer_rsrc_t srd, unsigned so) {
    asm volatile("buffer_load_dword %0, %1, %2 offen lds" :: "v"(v), "s"(srd), "s"(so) : "memory");
}
template <int W> DI void wait_q(u32x4 (&q)[8]) {
    asm volatile("s_waitcnt vmcnt(%8)"
                 : "+v"(q[0]), "+v"(q[1]), "+v"(q[2]), "+v"(q[3]), "+v"(q[4]), "+v"(q[5]), "+v"(q[6]), "+v"(q[7]) : "n"(W) : "memory");
}
DI void dequant_pair(u32x4 (&o)[4], int kk, const u32x4& qa, const u32x4& qb, const float (&S)[4], const float (&C)[4]) {
    float t0, t1, t2, t3, t4, t5, t6, t7;
    asm volatile(
        "v_fma_mix_f32 %4, %12, %20, %24 op_sel_hi:[1,0,0]\n\t"
        "v_fma_mix_f32 %5, %16, %20, %24 op_sel_hi:[1,0,0]\n\t"
        "v_fma_mix_f32 %6, %13, %21, %25 op_sel_hi:[1,0,0]\n\t"
        "v_fma_mix_f32 %7, %17, %21, %25 op_sel_hi:[1,0,0]\n\t"
        "v_fma_mix_f32 %8, %14, %22, %26 op_sel_hi:[1,0,0]\n\t"
        "v_fma_mix_f32 %9, %18, %22, %26 op_sel_hi:[1,0,0]\n\t"
        "v_fma_mix_f32 %10, %15, %23, %27 op_sel_hi:[1,0,0]\n\t"
        "v_fma_mix_f32 %11, %19, %23, %27 op_sel_hi:[1,0,0]\n\t"
        "v_cvt_pk_bf16_f32 %0, %4, %5\n\t"
        "v_cvt_pk_bf16_f32 %1, %6, %7\n\t"
        "v_cvt_pk_bf16_f32 %2, %8, %9\n\t"
        "v_cvt_pk_bf16_f32 %3, %10, %11"
        : "=&v"(o[0][kk]), "=&v"(o[1][kk]), "=&v"(o[2][kk]), "=&v"(o[3][kk]),
          "=&v"(t0), "=&v"(t1), "=&v"(t2), "=&v"(t3), "=&v"(t4), "=&v"(t5), "=&v"(t6), "=&v"(t7)
        : "v"(qa[0]), "v"(qa[1]), "v"(qa[2]), "v"(qa[3]), "v"(qb[0]), "v"(qb[1]), "v"(qb[2]), "v"(qb[3]),
          "v"(S[0]), "v"(S[1]), "v"(S[2]), "v"(S[3]), "v"(C[0]), "v"(C[1]), "v"(C[2]), "v"(C[3]));
}
DI void make_scales(float (&S)[4], float (&C)[4], const u32x2& s2, const u32x2& z2) {
#pragma unroll
    for (int j = 0; j < 4; ++j) {
        const float s = __uint_as_float((j & 1) ? (s2[j >> 1] & 0xffff0000u) : (s2[j >> 1] << 16));
        const float z = __uint_as_float((j & 1) ? (z2[j >> 1] & 0xffff0000u) : (z2[j >> 1] << 16));
        S[j] = fmul(s, 16777216.0f);
        C[j] = fmul(-z, s);
    }
}
struct PCtx { unsigned vq[8], vx[8], m0a, wa0, wa1; __amdgpu_buffer_rsrc_t srdQ, srdX; };
template <int J> DI void ld_x(const PCtx& c, unsigned sx) { dma16<(J & 3) * 1024>(c.vx[J], c.srdX, sx); }
template <bool ISSUE, bool NEWS>
DI void pstep(u32x4 (&q)[8], u32x4 (&qn)[8], float (&S)[4], float (&C)[4], u32x2& s2, u32x2& z2, const PCtx& c, int bufoff, int ns) {
    u32x4 o[4];
    const unsigned sq = (unsigned)ns * (BK * QROW), sx = (unsigned)ns * (BK * 2), mb = c.m0a + (unsigned)(ns & 3) * A_STAGE;
    if constexpr (ISSUE) { set_m0(mb); ld_q(qn[0], c.vq[0], c.srdQ, sq); ld_q(qn[1], c.vq[1], c.srdQ, sq); }
    wait_q<ISSUE ? 10 : 8>(q);
#pragma unroll
    for (int kk = 0; kk < 4; ++kk) {
        dequant_pair(o, kk, q[2 * kk], q[2 * kk + 1], S, C);
        if constexpr (ISSUE) { if (kk < 3) { ld_q(qn[2 * kk + 2], c.vq[2 * kk + 2], c.srdQ, sq); ld_q(qn[2 * kk + 3], c.vq[2 * kk + 3], c.srdQ, sq); } }
    }
    if constexpr (ISSUE) { ld_x<0>(c, sx); ld_x<1>(c, sx); }
#pragma unroll
    for (int j = 0; j < 4; ++j) *(LAS u32x4*)(uintptr_t)(((j >> 1) ? c.wa1 : c.wa0) + (unsigned)(j * 128 + bufoff)) = o[j];
    if constexpr (ISSUE) { ld_x<2>(c, sx); ld_x<3>(c, sx); set_m0(mb + 4096u); }
    if constexpr (NEWS) {
        asm volatile("" : "+v"(s2), "+v"(z2) :: "memory");
        make_scales(S, C, s2, z2);
    }
    if constexpr (ISSUE) { ld_x<4>(c, sx); ld_x<5>(c, sx); ld_x<6>(c, sx); ld_x<7>(c, sx); WAIT_V(16); } else WAIT_V(0);
    WAIT_L0();
    BAR();
}

struct Frag { bf16x8 x[4]; bf16x8 w[8]; };
typedef f32x4 Acc[4][8];
DI void read_frags(Frag& f, unsigned xa, unsigned, unsigned w0, unsigned w1, int bufoff) {
    f.x[0] = *(LAS const bf16x8*)(uintptr_t)(xa);
#pragma unroll
    for (int t = 0; t < 8; ++t)
        f.w[t] = *(LAS const bf16x8*)(uintptr_t)(((t & 1) ? w1 : w0) + (unsigned)(t * 2048 + bufoff));
#pragma unroll
    for (int m = 1; m < 4; ++m)
        f.x[m] = *(LAS const bf16x8*)(uintptr_t)(xa + (unsigned)(m * 2048));
}
DI void mma(Acc& acc, const Frag& f) {
#pragma unroll
    for (int m = 0; m < 4; ++m)
#pragma unroll
        for (int tt = 0; tt < 8; ++tt) {
            const int t = (m & 1) ? 7 - tt : tt;
            acc[m][t] = __builtin_amdgcn_mfma_f32_16x16x32_bf16(f.w[t], f.x[m], acc[m][t], 0, 0, 0);
        }
}
DI void interleave() {
#pragma unroll
    for (int i = 0; i < 12; ++i) {
        __builtin_amdgcn_sched_group_barrier(0x008, 2, 0);
        __builtin_amdgcn_sched_group_barrier(0x100, 1, 0);
    }
    __builtin_amdgcn_sched_group_barrier(0x008, 8, 0);
}

__global__ __launch_bounds__(512, 2)
void w4a16_kernel(const unsigned short* __restrict__ x, const int* __restrict__ qw, const unsigned short* __restrict__ sc,
                  const unsigned short* __restrict__ zr, unsigned short* __restrict__ out, char* __restrict__ ws) {
    __shared__ __attribute__((aligned(1024))) char smem[LDS_TOTAL];
    unsigned long long dg0;
#define RT(v) asm volatile("s_memrealtime %0\n\ts_waitcnt lgkmcnt(0)" : "=s"(v) :: "memory")
    RT(dg0);
    const unsigned lbase = (unsigned)(uintptr_t)(LAS char*)smem;
    const int lane = threadIdx.x & 63;
    const int wid = __builtin_amdgcn_readfirstlane(threadIdx.x >> 6);
    const int bid = blockIdx.x, xcd = bid & 7, idx = bid >> 3;
    const int tn0 = (xcd >> 1) * 7 + (xcd & 1) * 3;
    const __amdgpu_buffer_rsrc_t srdQ = __builtin_amdgcn_make_buffer_rsrc((void*)qw, (short)0, K_ * N_ * 4, 0x00020000);
    const __amdgpu_buffer_rsrc_t srdS = __builtin_amdgcn_make_buffer_rsrc((void*)sc, (short)0, 8 * N_ * 2, 0x00020000);
    const __amdgpu_buffer_rsrc_t srdZ = __builtin_amdgcn_make_buffer_rsrc((void*)zr, (short)0, 8 * N_ * 2, 0x00020000);
    if (idx >= 28) {
        if (wid == 0) {
            const __amdgpu_buffer_rsrc_t srdXh = __builtin_amdgcn_make_buffer_rsrc((void*)x, (short)0, M_ * K_ * 2, 0x00020000);
            const int h = idx - 28;
            const unsigned vq = (unsigned)((h * 16 + (lane >> 4)) * QROW + tn0 * 512 + (lane & 15) * 128);
            const unsigned vx = (unsigned)((xcd * 256 + h * 64 + lane) * XROW);
            const unsigned vs = lane < 16 ? (unsigned)((2 * h + (lane >> 3)) * N_ * 2 + tn0 * 256 + (lane & 7) * 128) : 0x80000000u;
            set_m0(lbase);
            touch(vs, srdS, 0u); touch(vs, srdZ, 0u);
            for (int s = CQ; s < NIT; ++s) {
                touch(vx, srdXh, (unsigned)(s * 128));
#pragma unroll
                for (int u = 0; u < 4; ++u) touch(vq, srdQ, (unsigned)(s * BK + u * 4) * QROW);
            }
            WAIT_V(0);
        }
        STAMP(21);
        STAMP_OUT();
        return;
    }
    int tm, tn;
    if (idx < 24) { tm = idx & 7; tn = tn0 + (xcd & 1) + (idx >> 3); }
    else { tm = (xcd & 1) * 4 + (idx - 24); tn = (xcd >> 1) * 7 + 3; }
    const int m0 = tm * BM, n0 = tn * BN;

    if (wid >= 4) {
        const int pw = wid - 4;
        PCtx c;
        c.srdQ = srdQ;
        c.srdX = __builtin_amdgcn_make_buffer_rsrc((void*)((const char*)x - 4096), (short)0, M_ * K_ * 2 + 4096, 0x00020000);
        const int kb = 2 * pw + (lane >> 5), nb = lane & 31;
#pragma unroll
        for (int i = 0; i < 8; ++i) c.vq[i] = (unsigned)((kb * 8 + i) * QROW + (n0 + 4 * nb) * 4);
        const unsigned vs = (unsigned)((n0 + 4 * nb) * 2);
        u32x4 q0[8], q1[8]; u32x2 s2, z2;
        ld8(s2, vs, srdS, 0u); ld8(z2, vs, srdZ, 0u);
#pragma unroll
        for (int i = 0; i < 8; ++i) ld_q(q0[i], c.vq[i], c.srdQ, 0u);
#pragma unroll
        for (int j = 0; j < 8; ++j) {
            const int row = pw * 64 + j * 8 + (lane >> 3);
            c.vx[j] = (unsigned)((m0 + row) * XROW + (((lane & 7) ^ ((row >> 1) & 7)) * 16) + 4096 - (j & 3) * 1024);
        }
        c.m0a = lbase + LDS_A + pw * 8192;
        set_m0(c.m0a);
        ld_x<0>(c, 0u); ld_x<1>(c, 0u); ld_x<2>(c, 0u); ld_x<3>(c, 0u);
        set_m0(c.m0a + 4096u);
        ld_x<4>(c, 0u); ld_x<5>(c, 0u); ld_x<6>(c, 0u); ld_x<7>(c, 0u);
        const int rb = 32 * (nb >> 3) + 16 * (nb & 1) + 4 * ((nb >> 1) & 3);
        const int f = ((rb >> 1) & 7) ^ ((rb >> 4) & 1);
        c.wa0 = lbase + (unsigned)(LDS_B + rb * 128 + ((kb ^ f) * 16));
        c.wa1 = c.wa0 ^ 16u;
        STAMP(1);
        float S[4], C[4];
        asm volatile("s_waitcnt vmcnt(16)" : "+v"(s2), "+v"(z2) :: "memory");
        make_scales(S, C, s2, z2);
        for (int b = 0; b < NIT - 2; b += 2) {
            ld8(s2, vs, srdS, (unsigned)((b / 2 + 1) * N_ * 2)); ld8(z2, vs, srdZ, (unsigned)((b / 2 + 1) * N_ * 2));
            pstep<true, false>(q0, q1, S, C, s2, z2, c, 0, b + 1);
            pstep<true, true>(q1, q0, S, C, s2, z2, c, B_STAGE, b + 2);
        }
        pstep<true, false>(q0, q1, S, C, s2, z2, c, 0, NIT - 1);
        pstep<false, false>(q1, q0, S, C, s2, z2, c, B_STAGE, 0);
        STAMP(17);
        BAR();
    } else {
        const int w = wid;
        {
            constexpr int NX0 = 2048, NQ = CQ * 1024, TOT = NX0 + NQ + (CQ - 1) * 256;
            static_assert(TOT <= 64 * 112, "one load per wave");
            const int l = lane * 112 + idx * 4 + w;
            if (l < TOT) {
                const int lq = l - NX0, lg = lq - NQ;
                const char* p = lq < 0 ? (const char*)x + (size_t)l * XROW
                              : lg < 0 ? (const char*)qw + (size_t)(lq >> 4) * QROW + tn0 * 512 + (lq & 15) * 128
                                       : (const char*)x + (size_t)(xcd * 256 + (lg & 255)) * XROW + (1 + (lg >> 8)) * 128;
                const unsigned v = *(const volatile unsigned*)p;
                asm volatile("" :: "v"(v));
            }
        }
        const int fr = lane & 15, fq = lane >> 4;
        const unsigned sw = (unsigned)((fq ^ (fr >> 1)) * 16);
        const unsigned xa0 = lbase + (unsigned)(LDS_A + (w * 64 + fr) * 128) + sw;
        unsigned w00 = lbase + (unsigned)(LDS_B + fr * 128) + sw;
        unsigned w01 = w00 ^ 16u, w10 = w00 ^ 64u, w11 = w00 ^ 80u;
#define XQ(a) 0u
        asm volatile("" : "+v"(w00), "+v"(w01), "+v"(w10), "+v"(w11));
        Acc acc;
#pragma unroll
        for (int m = 0; m < (int)(sizeof(acc) / sizeof(acc[0])); ++m)
#pragma unroll
            for (int t = 0; t < (int)(sizeof(acc[0]) / sizeof(acc[0][0])); ++t) { acc[m][t] = 0.f; asm volatile("" : "+v"(acc[m][t])); }
        Frag f0, f1;
        asm volatile("" :: "s"(out));
        BAR();
        STAMP(2);
        read_frags(f0, xa0, XQ(xa0), w00, w01, 0);
        for (int it = 0; it < NIT; it += 2) {
            unsigned xe = xa0 + (unsigned)((it & 2) * A_STAGE);
            unsigned xn = xa0 + (unsigned)(((it + 2) & 2) * A_STAGE);
            asm volatile("" : "+v"(xe), "+v"(xn));
            SB0();
            read_frags(f1, xe ^ 64u, XQ(xe ^ 64u), w10, w11, 0);
            mma(acc, f0);
            interleave();
            SB0();
            WAIT_L0();
            BAR();
            SB0();
            read_frags(f0, xe + A_STAGE, XQ(xe) + A_STAGE, w00, w01, B_STAGE);
            mma(acc, f1);
            interleave();
            SB0();
            read_frags(f1, (xe ^ 64u) + A_STAGE, XQ(xe ^ 64u) + A_STAGE, w10, w11, B_STAGE);
            mma(acc, f0);
            interleave();
            SB0();
            WAIT_L0();
            BAR();
            SB0();
            read_frags(f0, xn, XQ(xn), w00, w01, 0);
            mma(acc, f1);
            interleave();
            SB0();
        }
        STAMP(19);
        const unsigned eb = lbase + (unsigned)(A_STAGE + w * (64 * OROW));
        const unsigned er = eb + (unsigned)((lane >> 4) * OROW + (lane & 15) * 16);
        const __amdgpu_buffer_rsrc_t srdO = __builtin_amdgcn_make_buffer_rsrc((void*)out, (short)0, M_ * N_ * 2, 0x00020000);
        const int ovo = (lane >> 4) * (N_ * 2) + (lane & 15) * 16;
        const int oso = ((m0 + w * 64) * N_ + n0) * 2;
        const unsigned ew = eb + (unsigned)(fr * OROW + fq * 16);
#pragma unroll
        for (int m = 0; m < 4; ++m) {
#pragma unroll
            for (int pp = 0; pp < 4; ++pp) {
                u32x4 v;
                v[0] = pack_bf16(acc[m][2 * pp][0], acc[m][2 * pp][1]);
                v[1] = pack_bf16(acc[m][2 * pp][2], acc[m][2 * pp][3]);
                v[2] = pack_bf16(acc[m][2 * pp + 1][0], acc[m][2 * pp + 1][1]);
                v[3] = pack_bf16(acc[m][2 * pp + 1][2], acc[m][2 * pp + 1][3]);
                *(LAS u32x4*)(uintptr_t)(ew + (unsigned)(m * 16 * OROW + pp * 64)) = v;
            }
#pragma unroll
            for (int i = 0; i < 4; ++i) {
                const u32x4 v = *(LAS const u32x4*)(uintptr_t)(er + (unsigned)((m * 16 + i * 4) * OROW));
                __builtin_amdgcn_raw_buffer_store_b128(v, srdO, ovo, oso + (m * 16 + i * 4) * (N_ * 2), STAUX);
            }
        }
        STAMP(20);
        WAIT_V(0);
        { unsigned long long base = dg0, extra = 0;
          if (TMODE >= 1) { const unsigned long long ts = __hip_atomic_load((unsigned long long*)ws, __ATOMIC_RELAXED, __HIP_MEMORY_SCOPE_AGENT);
              base = ((unsigned long long)__builtin_amdgcn_readfirstlane((unsigned)(ts >> 32)) << 32) | (unsigned)__builtin_amdgcn_readfirstlane((unsigned)ts);
              if (dg0 - base > 100000ull) base = dg0;
              if (TMODE == 2) extra = 8 * (dg0 - base); }
          unsigned long long now; do { __builtin_amdgcn_s_sleep(4); RT(now); } while (now - base < 4000 + extra); }
    }
}
__global__ void tiny_kernel(char* ws) {
    unsigned long long t = __builtin_amdgcn_s_memrealtime();
    if (threadIdx.x == 0 && blockIdx.x == 0) __hip_atomic_store((unsigned long long*)ws, t, __ATOMIC_RELAXED, __HIP_MEMORY_SCOPE_AGENT);
}
}

extern "C" void kernel_launch(void* const* d_in, const int* in_sizes, int n_in,
                              void* d_out, int out_size, void* d_ws, size_t ws_size,
                              hipStream_t stream) {
    if (TMODE >= 1) tiny_kernel<<<TGRID, 64, 0, stream>>>((char*)d_ws);
    w4a16_kernel<<<256, 512, 0, stream>>>((const unsigned short*)d_in[0], (const int*)d_in[1],
        (const unsigned short*)d_in[2], (const unsigned short*)d_in[3], (unsigned short*)d_out, (char*)d_ws);
}
```

```cpp
#include <hip/hip_runtime.h>
#include <hip/hip_bf16.h>

typedef short bf16x8 __attribute__((ext_vector_type(8)));
typedef float f32x2 __attribute__((ext_vector_type(2)));
typedef float f32x4 __attribute__((ext_vector_type(4)));
typedef __bf16 bf16x2 __attribute__((ext_vector_type(2)));
typedef unsigned u32x4 __attribute__((ext_vector_type(4)));
typedef unsigned u32x2 __attribute__((ext_vector_type(2)));
#define LAS __attribute__((address_space(3)))
#define DI __device__ __forceinline__


namespace {
constexpr int M_ = 2048, N_ = 3584, K_ = 1024;
constexpr int BM = 256, BN = 128, BK = 64, NIT = K_ / BK;
constexpr int A_STAGE = BM * BK * 2;
constexpr int B_STAGE = BN * BK * 2;
constexpr int LDS_A = 0, LDS_B = 4 * A_STAGE;
constexpr int LDS_TOTAL = LDS_B + 2 * B_STAGE;
constexpr int QROW = N_ * 4;
constexpr int XROW = K_ * 2;
constexpr int CQ = 1;
constexpr int STAUX = 2;
constexpr int OROW = 272;

#define WAIT_V(n) asm volatile("s_waitcnt vmcnt(%0)" ::"n"(n) : "memory")
#define WAIT_L0() asm volatile("s_waitcnt lgkmcnt(0)" ::: "memory")
#define BAR() do { asm volatile("" ::: "memory"); __builtin_amdgcn_s_barrier(); asm volatile("" ::: "memory"); } while (0)
#define SB0() __builtin_amdgcn_sched_barrier(0)

DI unsigned pack_bf16(float lo, float hi) { return __builtin_bit_cast(unsigned, __builtin_convertvector((f32x2){lo, hi}, bf16x2)); }
DI float fmul(float a, float b) { float r; asm volatile("v_mul_f32 %0, %1, %2" : "=v"(r) : "v"(a), "v"(b)); return r; }
DI void ld_q(u32x4& q, unsigned vq, __amdgpu_buffer_rsrc_t srd, unsigned so) {
    asm volatile("buffer_load_dwordx4 %0, %1, %2, %3 offen" : "=v"(q) : "v"(vq), "s"(srd), "s"(so) : "memory");
}
DI void ld8(u32x2& q, unsigned v, __amdgpu_buffer_rsrc_t srd, unsigned so) {
    asm volatile("buffer_load_dwordx2 %0, %1, %2, %3 offen" : "=v"(q) : "v"(v), "s"(srd), "s"(so) : "memory");
}
DI void set_m0(unsigned m0v) { asm volatile("s_mov_b32 m0, %0\n\ts_nop 0" :: "s"(m0v) : "memory"); }
template <int OFF> DI void dma16(unsigned v, __amdgpu_buffer_rsrc_t srd, unsigned so) {
    asm volatile("buffer_load_dwordx4 %0, %1, %2 offen offset:%3 lds" :: "v"(v), "s"(srd), "s"(so), "n"(OFF) : "memory");
}
DI void touch(unsigned v, __amdgpu_buffer_rsrc_t srd, unsigned so) {
    asm volatile("buffer_load_dword %0, %1, %2 offen lds" :: "v"(v), "s"(srd), "s"(so) : "memory");
}
template <int W> DI void wait_q(u32x4 (&q)[8]) {
    asm volatile("s_waitcnt vmcnt(%8)"
                 : "+v"(q[0]), "+v"(q[1]), "+v"(q[2]), "+v"(q[3]), "+v"(q[4]), "+v"(q[5]), "+v"(q[6]), "+v"(q[7]) : "n"(W) : "memory");
}
DI void dequant_pair(u32x4 (&o)[4], int kk, const u32x4& qa, const u32x4& qb, const float (&S)[4], const float (&C)[4]) {
    float t0, t1, t2, t3, t4, t5, t6, t7;
    asm volatile(
        "v_fma_mix_f32 %4, %12, %20, %24 op_sel_hi:[1,0,0]\n\t"
        "v_fma_mix_f32 %5, %16, %20, %24 op_sel_hi:[1,0,0]\n\t"
        "v_fma_mix_f32 %6, %13, %21, %25 op_sel_hi:[1,0,0]\n\t"
        "v_fma_mix_f32 %7, %17, %21, %25 op_sel_hi:[1,0,0]\n\t"
        "v_fma_mix_f32 %8, %14, %22, %26 op_sel_hi:[1,0,0]\n\t"
        "v_fma_mix_f32 %9, %18, %22, %26 op_sel_hi:[1,0,0]\n\t"
        "v_fma_mix_f32 %10, %15, %23, %27 op_sel_hi:[1,0,0]\n\t"
        "v_fma_mix_f32 %11, %19, %23, %27 op_sel_hi:[1,0,0]\n\t"
        "v_cvt_pk_bf16_f32 %0, %4, %5\n\t"
        "v_cvt_pk_bf16_f32 %1, %6, %7\n\t"
        "v_cvt_pk_bf16_f32 %2, %8, %9\n\t"
        "v_cvt_pk_bf16_f32 %3, %10, %11"
        : "=&v"(o[0][kk]), "=&v"(o[1][kk]), "=&v"(o[2][kk]), "=&v"(o[3][kk]),
          "=&v"(t0), "=&v"(t1), "=&v"(t2), "=&v"(t3), "=&v"(t4), "=&v"(t5), "=&v"(t6), "=&v"(t7)
        : "v"(qa[0]), "v"(qa[1]), "v"(qa[2]), "v"(qa[3]), "v"(qb[0]), "v"(qb[1]), "v"(qb[2]), "v"(qb[3]),
          "v"(S[0]), "v"(S[1]), "v"(S[2]), "v"(S[3]), "v"(C[0]), "v"(C[1]), "v"(C[2]), "v"(C[3]));
}
DI void make_scales(float (&S)[4], float (&C)[4], const u32x2& s2, const u32x2& z2) {
#pragma unroll
    for (int j = 0; j < 4; ++j) {
        const float s = __uint_as_float((j & 1) ? (s2[j >> 1] & 0xffff0000u) : (s2[j >> 1] << 16));
        const float z = __uint_as_float((j & 1) ? (z2[j >> 1] & 0xffff0000u) : (z2[j >> 1] << 16));
        S[j] = fmul(s, 16777216.0f);
        C[j] = fmul(-z, s);
    }
}
struct PCtx { unsigned vq[8], vx[8], m0a, wa0, wa1; __amdgpu_buffer_rsrc_t srdQ, srdX; };
template <int J> DI void ld_x(const PCtx& c, unsigned sx) { dma16<(J & 3) * 1024>(c.vx[J], c.srdX, sx); }
template <bool ISSUE, bool NEWS, int XTRA>
DI void pstep(u32x4 (&q)[8], u32x4 (&qn)[8], float (&S)[4], float (&C)[4], u32x2& s2, u32x2& z2, const PCtx& c, int bufoff, int ns) {
    u32x4 o[4];
    const unsigned sq = (unsigned)ns * (BK * QROW), sx = (unsigned)ns * (BK * 2), mb = c.m0a + (unsigned)(ns & 3) * A_STAGE;
    if constexpr (ISSUE) { set_m0(mb); ld_q(qn[0], c.vq[0], c.srdQ, sq); ld_q(qn[1], c.vq[1], c.srdQ, sq); }
    wait_q<8 + XTRA + (ISSUE ? 2 : 0)>(q);
#pragma unroll
    for (int kk = 0; kk < 4; ++kk) {
        dequant_pair(o, kk, q[2 * kk], q[2 * kk + 1], S, C);
        if constexpr (ISSUE) { if (kk < 3) { ld_q(qn[2 * kk + 2], c.vq[2 * kk + 2], c.srdQ, sq); ld_q(qn[2 * kk + 3], c.vq[2 * kk + 3], c.srdQ, sq); } }
    }
    if constexpr (ISSUE) { ld_x<0>(c, sx); ld_x<1>(c, sx); }
#pragma unroll
    for (int j = 0; j < 4; ++j) *(LAS u32x4*)(uintptr_t)(((j >> 1) ? c.wa1 : c.wa0) + (unsigned)(j * 128 + bufoff)) = o[j];
    if constexpr (ISSUE) { ld_x<2>(c, sx); ld_x<3>(c, sx); set_m0(mb + 4096u); }
    if constexpr (NEWS) {
        asm volatile("" : "+v"(s2), "+v"(z2) :: "memory");
        make_scales(S, C, s2, z2);
    }
    if constexpr (ISSUE) { ld_x<4>(c, sx); ld_x<5>(c, sx); ld_x<6>(c, sx); ld_x<7>(c, sx); WAIT_V(16); } else WAIT_V(0);
    WAIT_L0();
    BAR();
}

struct Frag { bf16x8 x[4]; bf16x8 w[8]; };
typedef f32x4 Acc[4][8];
DI void read_frags(Frag& f, unsigned xa, unsigned w0, unsigned w1, int bufoff) {
    f.x[0] = *(LAS const bf16x8*)(uintptr_t)(xa);
#pragma unroll
    for (int t = 0; t < 8; ++t)
        f.w[t] = *(LAS const bf16x8*)(uintptr_t)(((t & 1) ? w1 : w0) + (unsigned)(t * 2048 + bufoff));
#pragma unroll
    for (int m = 1; m < 4; ++m)
        f.x[m] = *(LAS const bf16x8*)(uintptr_t)(xa + (unsigned)(m * 2048));
}
DI void mma(Acc& acc, const Frag& f) {
#pragma unroll
    for (int m = 0; m < 4; ++m)
#pragma unroll
        for (int tt = 0; tt < 8; ++tt) {
            const int t = (m & 1) ? 7 - tt : tt;
            acc[m][t] = __builtin_amdgcn_mfma_f32_16x16x32_bf16(f.w[t], f.x[m], acc[m][t], 0, 0, 0);
        }
}
DI void interleave() {
#pragma unroll
    for (int i = 0; i < 12; ++i) {
        __builtin_amdgcn_sched_group_barrier(0x008, 2, 0);
        __builtin_amdgcn_sched_group_barrier(0x100, 1, 0);
    }
    __builtin_amdgcn_sched_group_barrier(0x008, 8, 0);
}

__global__ __launch_bounds__(512, 2)
void w4a16_kernel(const unsigned short* __restrict__ x, const int* __restrict__ qw, const unsigned short* __restrict__ sc,
                  const unsigned short* __restrict__ zr, unsigned short* __restrict__ out) {
    __shared__ __attribute__((aligned(1024))) char smem[LDS_TOTAL];
    const unsigned lbase = (unsigned)(uintptr_t)(LAS char*)smem;
    const int lane = threadIdx.x & 63;
    const int wid = __builtin_amdgcn_readfirstlane(threadIdx.x >> 6);
    const int bid = blockIdx.x, xcd = bid & 7, idx = bid >> 3;
    const int tn0 = (xcd >> 1) * 7 + (xcd & 1) * 3;
    const __amdgpu_buffer_rsrc_t srdQ = __builtin_amdgcn_make_buffer_rsrc((void*)qw, (short)0, K_ * N_ * 4, 0x00020000);
    const __amdgpu_buffer_rsrc_t srdS = __builtin_amdgcn_make_buffer_rsrc((void*)sc, (short)0, 8 * N_ * 2, 0x00020000);
    const __amdgpu_buffer_rsrc_t srdZ = __builtin_amdgcn_make_buffer_rsrc((void*)zr, (short)0, 8 * N_ * 2, 0x00020000);
    if (idx >= 28) {
        if (wid == 0) {
            const __amdgpu_buffer_rsrc_t srdXh = __builtin_amdgcn_make_buffer_rsrc((void*)x, (short)0, M_ * K_ * 2, 0x00020000);
            const int h = idx - 28;
            const unsigned vq = (unsigned)((h * 16 + (lane >> 4)) * QROW + tn0 * 512 + (lane & 15) * 128);
            const unsigned vx = (unsigned)((xcd * 256 + h * 64 + lane) * XROW);
            const unsigned vs = lane < 16 ? (unsigned)((2 * h + (lane >> 3)) * N_ * 2 + tn0 * 256 + (lane & 7) * 128) : 0x80000000u;
            set_m0(lbase);
            touch(vs, srdS, 0u); touch(vs, srdZ, 0u);
            for (int s = CQ; s < NIT; ++s) {
                touch(vx, srdXh, (unsigned)(s * 128));
#pragma unroll
                for (int u = 0; u < 4; ++u) touch(vq, srdQ, (unsigned)(s * BK + u * 4) * QROW);
            }
            WAIT_V(0);
        }
        return;
    }
    int tm, tn;
    if (idx < 24) { tm = idx & 7; tn = tn0 + (xcd & 1) + (idx >> 3); }
    else { tm = (xcd & 1) * 4 + (idx - 24); tn = (xcd >> 1) * 7 + 3; }
    const int m0 = tm * BM, n0 = tn * BN;

    if (wid >= 4) {
        const int pw = wid - 4;
        PCtx c;
        c.srdQ = srdQ;
        c.srdX = __builtin_amdgcn_make_buffer_rsrc((void*)((const char*)x - 4096), (short)0, M_ * K_ * 2 + 4096, 0x00020000);
        const int kb = 2 * pw + (lane >> 5), nb = lane & 31;
#pragma unroll
        for (int i = 0; i < 8; ++i) c.vq[i] = (unsigned)((kb * 8 + i) * QROW + (n0 + 4 * nb) * 4);
        const unsigned vs = (unsigned)((n0 + 4 * nb) * 2);
        u32x4 q0[8], q1[8]; u32x2 s2, z2;
        ld8(s2, vs, srdS, 0u); ld8(z2, vs, srdZ, 0u);
#pragma unroll
        for (int i = 0; i < 8; ++i) ld_q(q0[i], c.vq[i], c.srdQ, 0u);
#pragma unroll
        for (int j = 0; j < 8; ++j) {
            const int row = pw * 64 + j * 8 + (lane >> 3);
            c.vx[j] = (unsigned)((m0 + row) * XROW + (((lane & 7) ^ ((row >> 1) & 7)) * 16) + 4096 - (j & 3) * 1024);
        }
        c.m0a = lbase + LDS_A + pw * 8192;
        set_m0(c.m0a);
        ld_x<0>(c, 0u); ld_x<1>(c, 0u); ld_x<2>(c, 0u); ld_x<3>(c, 0u);
        set_m0(c.m0a + 4096u);
        ld_x<4>(c, 0u); ld_x<5>(c, 0u); ld_x<6>(c, 0u); ld_x<7>(c, 0u);
        const int rb = 32 * (nb >> 3) + 16 * (nb & 1) + 4 * ((nb >> 1) & 3);
        const int f = ((rb >> 1) & 7) ^ ((rb >> 4) & 1);
        c.wa0 = lbase + (unsigned)(LDS_B + rb * 128 + ((kb ^ f) * 16));
        c.wa1 = c.wa0 ^ 16u;
        float S[4], C[4];
        asm volatile("s_waitcnt vmcnt(16)" : "+v"(s2), "+v"(z2) :: "memory");
        make_scales(S, C, s2, z2);
        for (int b = 0; b < NIT - 2; b += 2) {
            ld8(s2, vs, srdS, (unsigned)((b / 2 + 1) * N_ * 2)); ld8(z2, vs, srdZ, (unsigned)((b / 2 + 1) * N_ * 2));
            pstep<true, false, 2>(q0, q1, S, C, s2, z2, c, 0, b + 1);
            pstep<true, true, 0>(q1, q0, S, C, s2, z2, c, B_STAGE, b + 2);
        }
        pstep<true, false, 0>(q0, q1, S, C, s2, z2, c, 0, NIT - 1);
        pstep<false, false, 0>(q1, q0, S, C, s2, z2, c, B_STAGE, 0);
        BAR();
    } else {
        const int w = wid;
        {
            constexpr int NX0 = 2048, NQ = CQ * 1024, TOT = NX0 + NQ + (CQ - 1) * 256;
            static_assert(TOT <= 64 * 112, "one load per wave");
            const int l = lane * 112 + idx * 4 + w;
            if (l < TOT) {
                const int lq = l - NX0, lg = lq - NQ;
                const char* p = lq < 0 ? (const char*)x + (size_t)l * XROW
                              : lg < 0 ? (const char*)qw + (size_t)(lq >> 4) * QROW + tn0 * 512 + (lq & 15) * 128
                                       : (const char*)x + (size_t)(xcd * 256 + (lg & 255)) * XROW + (1 + (lg >> 8)) * 128;
                const unsigned v = *(const volatile unsigned*)p;
                asm volatile("" :: "v"(v));
            }
        }
        const int fr = lane & 15, fq = lane >> 4;
        const unsigned sw = (unsigned)((fq ^ (fr >> 1)) * 16);
        const unsigned xa0 = lbase + (unsigned)(LDS_A + (w * 64 + fr) * 128) + sw;
        unsigned w00 = lbase + (unsigned)(LDS_B + fr * 128) + sw;
        unsigned w01 = w00 ^ 16u, w10 = w00 ^ 64u, w11 = w00 ^ 80u;
        asm volatile("" : "+v"(w00), "+v"(w01), "+v"(w10), "+v"(w11));
        Acc acc;
#pragma unroll
        for (int m = 0; m < 4; ++m)
#pragma unroll
            for (int t = 0; t < 8; ++t) { acc[m][t] = 0.f; asm volatile("" : "+v"(acc[m][t])); }
        Frag f0, f1;
        asm volatile("" :: "s"(out));
        BAR();
        read_frags(f0, xa0, w00, w01, 0);
        for (int it = 0; it < NIT; it += 2) {
            unsigned xe = xa0 + (unsigned)((it & 2) * A_STAGE);
            unsigned xn = xa0 + (unsigned)(((it + 2) & 2) * A_STAGE);
            asm volatile("" : "+v"(xe), "+v"(xn));
            SB0();
            read_frags(f1, xe ^ 64u, w10, w11, 0);
            mma(acc, f0);
            interleave();
            SB0();
            WAIT_L0();
            BAR();
            SB0();
            read_frags(f0, xe + A_STAGE, w00, w01, B_STAGE);
            mma(acc, f1);
            interleave();
            SB0();
            read_frags(f1, (xe ^ 64u) + A_STAGE, w10, w11, B_STAGE);
            mma(acc, f0);
            interleave();
            SB0();
            WAIT_L0();
            BAR();
            SB0();
            read_frags(f0, xn, w00, w01, 0);
            mma(acc, f1);
            interleave();
            SB0();
        }
        const unsigned eb = lbase + (unsigned)(A_STAGE + w * (64 * OROW));
        const unsigned er = eb + (unsigned)((lane >> 4) * OROW + (lane & 15) * 16);
        const __amdgpu_buffer_rsrc_t srdO = __builtin_amdgcn_make_buffer_rsrc((void*)out, (short)0, M_ * N_ * 2, 0x00020000);
        const int ovo = (lane >> 4) * (N_ * 2) + (lane & 15) * 16;
        const int oso = ((m0 + w * 64) * N_ + n0) * 2;
        const unsigned ew = eb + (unsigned)(fr * OROW + fq * 16);
#pragma unroll
        for (int m = 0; m < 4; ++m) {
#pragma unroll
            for (int pp = 0; pp < 4; ++pp) {
                u32x4 v;
                v[0] = pack_bf16(acc[m][2 * pp][0], acc[m][2 * pp][1]);
                v[1] = pack_bf16(acc[m][2 * pp][2], acc[m][2 * pp][3]);
                v[2] = pack_bf16(acc[m][2 * pp + 1][0], acc[m][2 * pp + 1][1]);
                v[3] = pack_bf16(acc[m][2 * pp + 1][2], acc[m][2 * pp + 1][3]);
                *(LAS u32x4*)(uintptr_t)(ew + (unsigned)(m * 16 * OROW + pp * 64)) = v;
            }
#pragma unroll
            for (int i = 0; i < 4; ++i) {
                const u32x4 v = *(LAS const u32x4*)(uintptr_t)(er + (unsigned)((m * 16 + i * 4) * OROW));
                __builtin_amdgcn_raw_buffer_store_b128(v, srdO, ovo, oso + (m * 16 + i * 4) * (N_ * 2), STAUX);
            }
        }
    }
}
}

extern "C" void kernel_launch(void* const* d_in, const int* in_sizes, int n_in,
                              void* d_out, int out_size, void* d_ws, size_t ws_size,
                              hipStream_t stream) {
    w4a16_kernel<<<256, 512, 0, stream>>>((const unsigned short*)d_in[0], (const int*)d_in[1],
        (const unsigned short*)d_in[2], (const unsigned short*)d_in[3], (unsigned short*)d_out);
}
```
